```python
import torch

def _softmax(x, axis=-1):
    return torch.softmax(x, dim=axis)
def _gelu(x):
    return torch.nn.functional.gelu(x, approximate="tanh")
def _silu(x):
    return torch.nn.functional.silu(x)
def _sigmoid(x):
    return torch.sigmoid(x)
def _layernorm(x, axis=-1, eps=1e-5):
    m = x.mean(dim=axis, keepdim=True)
    v = x.var(dim=axis, keepdim=True, unbiased=False)
    return (x - m) / torch.sqrt(v + eps)
def _scatter_add(tgt, idx, src, axis=0):
    return tgt.clone().index_add_(axis, idx, src)
def _segment_sum(src, seg, n):
    out = torch.zeros((n,) + src.shape[1:], dtype=src.dtype)
    return out.index_add_(0, seg, src)
def _pad(x, pw):
    flat = tuple(v for pair in reversed(pw) for v in pair)
    return torch.nn.functional.pad(x, flat)

kernel_name = 'recurrent_scan__gqa_block_cached__dense_activation_gelu__decode'

def setup_inputs():

    _w = (lambda a: (lambda t: t.to(torch.bfloat16) if t.is_floating_point() else t)(torch.as_tensor(a)))
    def _A_setup():
        B, Sq, D = 8, 1, 448
        Hq, Hkv, Sk, Dh = 16, 8, 8192, 256
        assert Hq % Hkv == 0, (Hq, Hkv)
        return {
            "x": _w((lambda *s: torch.randn(*s, dtype=torch.bfloat16) * 0.05)(B, Sq, D)),
            "k_cache": _w((lambda *s: torch.randn(*s, dtype=torch.bfloat16) * 3.2)(B, Hkv, Sk, Dh)),
            "v_cache": _w((lambda *s: torch.randn(*s, dtype=torch.bfloat16) * 3.2)(B, Hkv, Sk, Dh)),
            "wq": _w((lambda *s: torch.randn(*s, dtype=torch.bfloat16) * 3.2)(D, Hq * Dh) * (D ** -0.5)),
            "wo": _w((lambda *s: torch.randn(*s, dtype=torch.bfloat16) * 3.2)(Hq * Dh, D) * ((Hq * Dh) ** -0.5)),
        }
    _A_in = _A_setup()

    def _B_setup():
        B, S, D, F = 8, 1, 448, 1792
        return {
            "x": _w((lambda *s: torch.randn(*s, dtype=torch.bfloat16) * 0.05)(B, S, D)),
            "w1": _w((lambda *s: torch.randn(*s, dtype=torch.bfloat16) * 3.2)(D, F) * (D ** -0.5)),
            "b1": _w((lambda *s: torch.randn(*s, dtype=torch.bfloat16) * 3.2)(F) * 0.01),
            "w2": _w((lambda *s: torch.randn(*s, dtype=torch.bfloat16) * 0.8)(F, D) * (F ** -0.5)),
            "b2": _w((lambda *s: torch.randn(*s, dtype=torch.bfloat16) * 0.8)(D) * 0.01),
        }
    _B_in = _B_setup()

    return {
        "A_x": _A_in["x"],
        "A_k_cache": _A_in["k_cache"],
        "A_v_cache": _A_in["v_cache"],
        "A_wq": _A_in["wq"],
        "A_wo": _A_in["wo"],
        "B_x": _B_in["x"],
        "B_w1": _B_in["w1"],
        "B_b1": _B_in["b1"],
        "B_w2": _B_in["w2"],
        "B_b2": _B_in["b2"],
    }

def reference(A_x, A_k_cache, A_v_cache, A_wq, A_wo, B_x, B_w1, B_b1, B_w2, B_b2):
    def _A_ref(x, k_cache, v_cache, wq, wo):
        B, Sq, D = x.shape
        Hq, Dh = 16, 256
        Hkv = k_cache.shape[1]
        rep = Hq // Hkv
        q = (lambda x, *d: x.permute(*d))((x @ wq).reshape(B, Sq, Hq, Dh), 0, 2, 1, 3)
        k = (lambda x, n, axis: torch.repeat_interleave(x, n, dim=axis))(k_cache, rep, axis=1)
        v = (lambda x, n, axis: torch.repeat_interleave(x, n, dim=axis))(v_cache, rep, axis=1)
        scores = torch.einsum("bhqd,bhkd->bhqk", q, k) * (Dh ** -0.5)
        p = _softmax(scores, axis=-1)
        o = torch.einsum("bhqk,bhkd->bhqd", p, v)
        return (lambda x, *d: x.permute(*d))(o, 0, 2, 1, 3).reshape(B, Sq, Hq * Dh) @ wo

    def _B_ref(x, w1, b1, w2, b2):
        h = torch.einsum("bsd,df->bsf", x, w1) + b1
        h = _gelu(h)
        return torch.einsum("bsf,fd->bsd", h, w2) + b2

    x = A_x
    xn = _layernorm(x, axis=-1)
    s = _A_ref(xn, A_k_cache, A_v_cache, A_wq, A_wo)
    g = _silu(_B_ref(xn, B_w1, B_b1, B_w2, B_b2))
    return x + s * g
```

```cpp
#include <hip/hip_runtime.h>
#include <hip/hip_bf16.h>
#include <stdint.h>

typedef unsigned v4u __attribute__((ext_vector_type(4)));
typedef float f32x4 __attribute__((ext_vector_type(4)));
typedef __attribute__((ext_vector_type(2))) __bf16 bf16x2;
#define GAS __attribute__((address_space(1)))

constexpr int B = 8, D = 448, HQ = 16, HKV = 8, SK = 8192, DH = 256, F = 1792;
constexpr int NSPLIT = 4, NW = 8, R = 2;
constexpr int KEYS_WG = SK / NSPLIT;
constexpr int TILES_W = KEYS_WG / 16 / NW;
#define ROUND_S 0

__device__ __forceinline__ float bf2f(unsigned short b) { return __builtin_bit_cast(float, (unsigned)b << 16); }
__device__ __forceinline__ float rbf(float f) { return (float)(__bf16)f; }
__device__ __forceinline__ unsigned short f2bf(float f) { return __builtin_bit_cast(unsigned short, (__bf16)f); }
__device__ __forceinline__ float lo16(unsigned u) { return __builtin_bit_cast(float, u << 16); }
__device__ __forceinline__ float hi16(unsigned u) { return __builtin_bit_cast(float, u & 0xffff0000u); }
__device__ __forceinline__ float dot2(unsigned k, unsigned q, float acc) {
  return __builtin_amdgcn_fdot2_f32_bf16(__builtin_bit_cast(bf16x2, k), __builtin_bit_cast(bf16x2, q), acc, false);
}
template <int CTRL> __device__ __forceinline__ float dpp(float x) {
  return __builtin_bit_cast(float, __builtin_amdgcn_mov_dpp(__builtin_bit_cast(int, x), CTRL, 0xf, 0xf, true));
}
constexpr int XOR1 = 0xB1, XOR2 = 0x4E, XOR7 = 0x141, XOR8 = 0x128;
__device__ __forceinline__ float x16_sum(float x) {
  auto s = __builtin_amdgcn_permlane16_swap(__float_as_uint(x), __float_as_uint(x), false, false);
  return __uint_as_float(s[0]) + __uint_as_float(s[1]);
}
__device__ __forceinline__ float x32_max(float x) {
  auto s = __builtin_amdgcn_permlane32_swap(__float_as_uint(x), __float_as_uint(x), false, false);
  return fmaxf(__uint_as_float(s[0]), __uint_as_float(s[1]));
}
__device__ __forceinline__ float x32_sum(float x) {
  auto s = __builtin_amdgcn_permlane32_swap(__float_as_uint(x), __float_as_uint(x), false, false);
  return __uint_as_float(s[0]) + __uint_as_float(s[1]);
}
__device__ __forceinline__ v4u ldnt(const GAS v4u* p) { return __builtin_nontemporal_load(p); }

constexpr size_t WS_Q = 0;
constexpr size_t WS_H = WS_Q + 8 * 4096 * 2;
constexpr size_t WS_PO = 1 << 20;
constexpr size_t WS_PML = WS_PO + (size_t)64 * NSPLIT * 2 * 256 * 4;
constexpr size_t WS_O = WS_PML + (size_t)64 * NSPLIT * 4 * 4 + 4096;

__device__ __forceinline__ void layernorm_all(const unsigned short* x, float* xn) {
  const int tid = threadIdx.x, wave = tid >> 6, lane = tid & 63, nw = blockDim.x >> 6;
  for (int b = wave; b < B; b += nw) {
    float v[7]; float s = 0.f;
#pragma unroll
    for (int j = 0; j < 7; ++j) { v[j] = bf2f(x[b * D + lane + 64 * j]); s += v[j]; }
#pragma unroll
    for (int m = 32; m >= 1; m >>= 1) s += __shfl_xor(s, m);
    const float mean = s * (1.f / D);
    float q = 0.f;
#pragma unroll
    for (int j = 0; j < 7; ++j) { const float d = v[j] - mean; q += d * d; }
#pragma unroll
    for (int m = 32; m >= 1; m >>= 1) q += __shfl_xor(q, m);
    const float var = rbf(q * (1.f / D));
    const float mb = rbf(mean);
    const float den = rbf(sqrtf(rbf(var + 1e-5f)));
#pragma unroll
    for (int j = 0; j < 7; ++j) xn[b * D + lane + 64 * j] = rbf(rbf(v[j] - mb) / den);
  }
}

__global__ void __launch_bounds__(256) k_pro(const unsigned short* x, const unsigned short* wq, const unsigned short* w1,
                                             const unsigned short* b1, unsigned short* q, unsigned short* h) {
  __shared__ float xn[B * D];
  __shared__ float red[64][32 * 8 + 8];
  layernorm_all(x, xn);
  __syncthreads();
  const int tid = threadIdx.x;
  const int cb = blockIdx.x;
  const bool isq = cb < 128;
  const unsigned short* W = isq ? wq : w1;
  const int N = isq ? 4096 : F;
  const int col0 = (isq ? cb : cb - 128) * 32;
  const int cq = tid & 3, kg = tid >> 2;
  float acc[8][8];
#pragma unroll
  for (int b = 0; b < 8; ++b)
#pragma unroll
    for (int j = 0; j < 8; ++j) acc[b][j] = 0.f;
  v4u wv[7];
#pragma unroll
  for (int r = 0; r < 7; ++r) wv[r] = *(const v4u*)(W + (size_t)(kg + 64 * r) * N + col0 + cq * 8);
#pragma unroll
  for (int r = 0; r < 7; ++r) {
    float wf[8];
#pragma unroll
    for (int j = 0; j < 4; ++j) { wf[2 * j] = lo16(wv[r][j]); wf[2 * j + 1] = hi16(wv[r][j]); }
#pragma unroll
    for (int b = 0; b < 8; ++b) { const float xv = xn[b * D + kg + 64 * r];
#pragma unroll
      for (int j = 0; j < 8; ++j) acc[b][j] = fmaf(xv, wf[j], acc[b][j]); }
  }
#pragma unroll
  for (int b = 0; b < 8; ++b)
#pragma unroll
    for (int j = 0; j < 8; ++j) { float v = acc[b][j]; v += __shfl_xor(v, 4); v += __shfl_xor(v, 8); v += __shfl_xor(v, 16); v += __shfl_xor(v, 32); acc[b][j] = v; }
  const int wave = tid >> 6, lane = tid & 63;
  if (lane < 4) {
#pragma unroll
    for (int b = 0; b < 8; ++b)
#pragma unroll
      for (int j = 0; j < 8; ++j) red[wave][b * 32 + lane * 8 + j] = acc[b][j];
  }
  __syncthreads();
  { const int b = tid >> 5, c = tid & 31;
    float v = red[0][tid] + red[1][tid] + red[2][tid] + red[3][tid];
    v = rbf(v);
    if (isq) q[b * 4096 + col0 + c] = f2bf(v);
    else { float t = rbf(v + bf2f(b1[col0 + c]));
      const float u = 0.7978845608028654f * (t + 0.044715f * t * t * t);
      const float g = 0.5f * t * (1.f + tanhf(u));
      h[b * F + col0 + c] = f2bf(g); }
  }
}

__global__ void __launch_bounds__(NW * 64, 2) k_attn(const unsigned short* Kc, const unsigned short* Vc, const unsigned short* q,
                                                     float* po, float* pml) {
  __shared__ __attribute__((aligned(16))) float mrg[NW][2][DH + 4];
  const int tid = threadIdx.x, lane = tid & 63, w = __builtin_amdgcn_readfirstlane(tid >> 6);
  const int bid = blockIdx.x;
  const int kvh = bid & 7, rest = bid >> 3, sp = rest & (NSPLIT - 1), b = rest / NSPLIT;
  const int pair = b * HKV + kvh;
  const int c = lane & 31, j16 = lane & 15;
  const size_t base = ((size_t)pair * SK + (size_t)sp * KEYS_WG) * DH;
  const GAS v4u* kp = (const GAS v4u*)(Kc + base) + (size_t)w * 512 + lane;
  const GAS v4u* vp = (const GAS v4u*)(Vc + base) + (size_t)w * 512 + lane;
  constexpr int TSTR = NW * 512;
  v4u Kb[R][8], Vb[R][8];
#pragma unroll
  for (int r = 0; r < R; ++r) {
#pragma unroll
    for (int i = 0; i < 8; ++i) Kb[r][i] = ldnt(kp + r * TSTR + i * 64);
#pragma unroll
    for (int i = 0; i < 8; ++i) Vb[r][i] = ldnt(vp + r * TSTR + i * 64);
  }
  __builtin_amdgcn_sched_barrier(0);
  v4u qv[2];
#pragma unroll
  for (int h = 0; h < 2; ++h) qv[h] = *(const v4u*)(q + (size_t)b * 4096 + (kvh * 2 + h) * DH + c * 8);
  asm volatile("" : "+v"(qv[0]), "+v"(qv[1]));
  float acc[2][8];
#pragma unroll
  for (int h = 0; h < 2; ++h)
#pragma unroll
    for (int d = 0; d < 8; ++d) acc[h][d] = 0.f;
  float m = -INFINITY, l = 0.f;
  constexpr float C2 = 0.0625f * 1.4426950408889634f;

#define TR_STEP(HALF, CTRL, BIT) _Pragma("unroll") for (int u = 0; u < (HALF); ++u) { \
        const float keep = (lane & (BIT)) ? x[u + (HALF)] : x[u]; const float send = (lane & (BIT)) ? x[u] : x[u + (HALF)]; \
        x[u] = keep + dpp<CTRL>(send); }
#define PV(I) { float vf[8]; _Pragma("unroll") for (int k = 0; k < 4; ++k) { vf[2 * k] = lo16(Vb[r][I][k]); vf[2 * k + 1] = hi16(Vb[r][I][k]); } \
        const float p0 = dpp<0x150 + I>(p), p1 = dpp<0x158 + I>(p); \
        _Pragma("unroll") for (int d = 0; d < 8; ++d) { acc[0][d] = fmaf(p0, vf[d], acc[0][d]); acc[1][d] = fmaf(p1, vf[d], acc[1][d]); } }
#define TILE(LOADNEXT) { \
      float x[16]; \
      _Pragma("unroll") for (int h = 0; h < 2; ++h) \
        _Pragma("unroll") for (int i = 0; i < 8; ++i) { float d = 0.f; \
          _Pragma("unroll") for (int k = 0; k < 4; ++k) d = dot2(Kb[r][i][k], qv[h][k], d); \
          x[h * 8 + i] = d; } \
      if (LOADNEXT) { _Pragma("unroll") for (int i = 0; i < 8; ++i) Kb[r][i] = ldnt(kn + r * TSTR + i * 64); } \
      __builtin_amdgcn_sched_barrier(0); \
      TR_STEP(8, XOR8, 8) TR_STEP(4, XOR7, 4) TR_STEP(2, XOR2, 2) TR_STEP(1, XOR1, 1) \
      float s = x16_sum(x[0]); \
      if (ROUND_S) s = rbf(s); \
      s *= C2; \
      if (__any(s > m)) { \
        float pm = s; pm = fmaxf(pm, dpp<XOR1>(pm)); pm = fmaxf(pm, dpp<XOR2>(pm)); pm = fmaxf(pm, dpp<XOR7>(pm)); pm = x32_max(pm); \
        const float mn = fmaxf(m, pm); \
        const float alpha = __builtin_amdgcn_exp2f(m - mn); \
        m = mn; l *= alpha; \
        const float a0 = dpp<0x150>(alpha), a1 = dpp<0x158>(alpha); \
        _Pragma("unroll") for (int d = 0; d < 8; ++d) { acc[0][d] *= a0; acc[1][d] *= a1; } \
      } \
      const float p = __builtin_amdgcn_exp2f(s - m); \
      l += p; \
      PV(0) PV(1) PV(2) PV(3) PV(4) PV(5) PV(6) PV(7) \
      if (LOADNEXT) { _Pragma("unroll") for (int i = 0; i < 8; ++i) Vb[r][i] = ldnt(vn + r * TSTR + i * 64); } \
      __builtin_amdgcn_sched_barrier(0); \
    }
  const GAS v4u* kn = kp + (size_t)R * TSTR;
  const GAS v4u* vn = vp + (size_t)R * TSTR;
#pragma unroll 1
  for (int t0 = 0; t0 < TILES_W - R; t0 += R) {
#pragma unroll
    for (int r = 0; r < R; ++r) TILE(true)
    kn += R * TSTR; vn += R * TSTR;
  }
#pragma unroll
  for (int r = 0; r < R; ++r) TILE(false)
#pragma unroll
  for (int h = 0; h < 2; ++h)
#pragma unroll
    for (int d = 0; d < 8; ++d) acc[h][d] = x32_sum(acc[h][d]);
  l += dpp<XOR1>(l); l += dpp<XOR2>(l); l += dpp<XOR7>(l); l = x32_sum(l);
  if (lane < 32) {
#pragma unroll
    for (int h = 0; h < 2; ++h) {
      *(f32x4*)&mrg[w][h][4 + c * 8] = f32x4{acc[h][0], acc[h][1], acc[h][2], acc[h][3]};
      *(f32x4*)&mrg[w][h][4 + c * 8 + 4] = f32x4{acc[h][4], acc[h][5], acc[h][6], acc[h][7]};
    }
    if ((lane & 7) == 0 && lane < 16) { mrg[w][lane >> 3][0] = m; mrg[w][lane >> 3][1] = l; }
  }
  __syncthreads();
  { const int h = tid >> 8, d = tid & 255;
    float M = -INFINITY;
#pragma unroll
    for (int ww = 0; ww < NW; ++ww) M = fmaxf(M, mrg[ww][h][0]);
    float L = 0.f, o = 0.f;
#pragma unroll
    for (int ww = 0; ww < NW; ++ww) { const float wt = __builtin_amdgcn_exp2f(mrg[ww][h][0] - M); L = fmaf(wt, mrg[ww][h][1], L); o = fmaf(wt, mrg[ww][h][4 + d], o); }
    const size_t slot = ((size_t)pair * NSPLIT + sp) * 2 + h;
    po[slot * DH + d] = o;
    if (d == 0) { pml[slot * 2] = M; pml[slot * 2 + 1] = L; }
  }
}

__global__ void __launch_bounds__(512) k_comb(const float* po, const float* pml, float* o) {
  const int pair = blockIdx.x, tid = threadIdx.x, h = tid >> 8, d = tid & 255;
  float M = -INFINITY;
#pragma unroll
  for (int s = 0; s < NSPLIT; ++s) M = fmaxf(M, pml[((pair * NSPLIT + s) * 2 + h) * 2]);
  float L = 0.f, a = 0.f;
#pragma unroll
  for (int s = 0; s < NSPLIT; ++s) { const int slot = (pair * NSPLIT + s) * 2 + h; const float wt = __builtin_amdgcn_exp2f(pml[slot * 2] - M);
    L = fmaf(wt, pml[slot * 2 + 1], L); a = fmaf(wt, po[(size_t)slot * DH + d], a); }
  const int b = pair >> 3, kvh = pair & 7;
  o[b * 4096 + (kvh * 2 + h) * DH + d] = rbf(a / L);
}

__global__ void __launch_bounds__(512) k_out(const float* o, const unsigned short* h, const unsigned short* wo, const unsigned short* w2,
                                             const unsigned short* b2, const unsigned short* x, unsigned short* out) {
  __shared__ float red[8][2][64];
  const int b = blockIdx.x / 7, cbk = blockIdx.x % 7, tid = threadIdx.x, cq = tid & 7, kg = tid >> 3, wave = tid >> 6, lane = tid & 63;
  const int col0 = cbk * 64 + cq * 8;
  float a1[8], a2[8];
#pragma unroll
  for (int j = 0; j < 8; ++j) { a1[j] = 0.f; a2[j] = 0.f; }
#pragma unroll 8
  for (int k = kg; k < 4096; k += 64) {
    const v4u wv = *(const v4u*)(wo + (size_t)k * D + col0); const float ov = o[b * 4096 + k];
#pragma unroll
    for (int j = 0; j < 4; ++j) { a1[2 * j] = fmaf(ov, lo16(wv[j]), a1[2 * j]); a1[2 * j + 1] = fmaf(ov, hi16(wv[j]), a1[2 * j + 1]); }
  }
#pragma unroll 7
  for (int k = kg; k < F; k += 64) {
    const v4u wv = *(const v4u*)(w2 + (size_t)k * D + col0); const float hv = bf2f(h[b * F + k]);
#pragma unroll
    for (int j = 0; j < 4; ++j) { a2[2 * j] = fmaf(hv, lo16(wv[j]), a2[2 * j]); a2[2 * j + 1] = fmaf(hv, hi16(wv[j]), a2[2 * j + 1]); }
  }
#pragma unroll
  for (int j = 0; j < 8; ++j) {
    float v = a1[j]; v += __shfl_xor(v, 8); v += __shfl_xor(v, 16); v += __shfl_xor(v, 32); a1[j] = v;
    float u = a2[j]; u += __shfl_xor(u, 8); u += __shfl_xor(u, 16); u += __shfl_xor(u, 32); a2[j] = u;
  }
  if (lane < 8) {
#pragma unroll
    for (int j = 0; j < 8; ++j) { red[wave][0][lane * 8 + j] = a1[j]; red[wave][1][lane * 8 + j] = a2[j]; }
  }
  __syncthreads();
  if (tid < 64) {
    float s = 0.f, y = 0.f;
#pragma unroll
    for (int ww = 0; ww < 8; ++ww) { s += red[ww][0][tid]; y += red[ww][1][tid]; }
    const int col = cbk * 64 + tid;
    s = rbf(s);
    y = rbf(rbf(y) + bf2f(b2[col]));
    const float g = rbf(y / (1.f + __expf(-y)));
    out[b * D + col] = f2bf(bf2f(x[b * D + col]) + rbf(s * g));
  }
}

extern "C" void kernel_launch(void* const* d_in, const int* in_sizes, int n_in, void* d_out, int out_size, void* d_ws, size_t ws_size,
                              hipStream_t stream) {
  const unsigned short* x = (const unsigned short*)d_in[0];
  const unsigned short* Kc = (const unsigned short*)d_in[1];
  const unsigned short* Vc = (const unsigned short*)d_in[2];
  const unsigned short* wq = (const unsigned short*)d_in[3];
  const unsigned short* wo = (const unsigned short*)d_in[4];
  const unsigned short* w1 = (const unsigned short*)d_in[6];
  const unsigned short* b1 = (const unsigned short*)d_in[7];
  const unsigned short* w2 = (const unsigned short*)d_in[8];
  const unsigned short* b2 = (const unsigned short*)d_in[9];
  char* ws = (char*)d_ws;
  unsigned short* q = (unsigned short*)(ws + WS_Q);
  unsigned short* h = (unsigned short*)(ws + WS_H);
  float* po = (float*)(ws + WS_PO);
  float* pml = (float*)(ws + WS_PML);
  float* o = (float*)(ws + WS_O);
  k_pro<<<184, 256, 0, stream>>>(x, wq, w1, b1, q, h);
  k_attn<<<64 * NSPLIT, NW * 64, 0, stream>>>(Kc, Vc, q, po, pml);
  k_comb<<<64, 512, 0, stream>>>(po, pml, o);
  k_out<<<56, 512, 0, stream>>>(o, h, wo, w2, b2, x, (unsigned short*)d_out);
}
```

```cpp
#include <hip/hip_runtime.h>
#include <hip/hip_bf16.h>
#include <stdint.h>

typedef unsigned v4u __attribute__((ext_vector_type(4)));
typedef unsigned v2u __attribute__((ext_vector_type(2)));
typedef float f32x4 __attribute__((ext_vector_type(4)));
typedef float f32x2 __attribute__((ext_vector_type(2)));
typedef __attribute__((ext_vector_type(2))) __bf16 bf16x2;
typedef unsigned long long u64;
#define GAS __attribute__((address_space(1)))
#define RLX_AGENT __ATOMIC_RELAXED, __HIP_MEMORY_SCOPE_AGENT

constexpr int B = 8, D = 448, HKV = 8, SK = 8192, DH = 256, F = 1792;
constexpr int NWG = 256, NW = 8, NTHR = 512;
constexpr int KEYS_WG = 2048, NT = KEYS_WG / 8 / NW;
constexpr unsigned SPIN_CAP = 1u << 22;

constexpr unsigned WS_EPOCH = 0;
constexpr unsigned WS_QG = 4096;
constexpr unsigned WS_HG = WS_QG + 8 * 8 * 256 * 8;
constexpr unsigned WS_PG = WS_HG + 8 * 896 * 8;
constexpr unsigned PG_STRIDE = 576;
constexpr unsigned WS_SG = WS_PG + 256 * PG_STRIDE * 8;
constexpr unsigned WS_END = WS_SG + 8 * 32 * 448 * 16;

__device__ __forceinline__ float bf2f(unsigned short b) { return __builtin_bit_cast(float, (unsigned)b << 16); }
__device__ __forceinline__ float rbf(float f) { return (float)(__bf16)f; }
__device__ __forceinline__ unsigned f2bf(float f) { return (unsigned)__builtin_bit_cast(unsigned short, (__bf16)f); }
__device__ __forceinline__ float lo16(unsigned u) { return __builtin_bit_cast(float, u << 16); }
__device__ __forceinline__ float hi16(unsigned u) { return __builtin_bit_cast(float, u & 0xffff0000u); }
__device__ __forceinline__ float dot2(unsigned k, unsigned q, float acc) {
  return __builtin_amdgcn_fdot2_f32_bf16(__builtin_bit_cast(bf16x2, k), __builtin_bit_cast(bf16x2, q), acc, false);
}
template <int CTRL> __device__ __forceinline__ float dpp(float x) {
  return __builtin_bit_cast(float, __builtin_amdgcn_mov_dpp(__builtin_bit_cast(int, x), CTRL, 0xf, 0xf, true));
}
constexpr int XOR1 = 0xB1, XOR2 = 0x4E, XOR7 = 0x141, XOR8 = 0x128;
__device__ __forceinline__ float x16_sum(float x) {
  auto s = __builtin_amdgcn_permlane16_swap(__float_as_uint(x), __float_as_uint(x), false, false);
  return __uint_as_float(s[0]) + __uint_as_float(s[1]);
}
__device__ __forceinline__ float x32_sum(float x) {
  auto s = __builtin_amdgcn_permlane32_swap(__float_as_uint(x), __float_as_uint(x), false, false);
  return __uint_as_float(s[0]) + __uint_as_float(s[1]);
}
__device__ __forceinline__ float x16_max(float x) {
  auto s = __builtin_amdgcn_permlane16_swap(__float_as_uint(x), __float_as_uint(x), false, false);
  return fmaxf(__uint_as_float(s[0]), __uint_as_float(s[1]));
}
__device__ __forceinline__ float x32_max(float x) {
  auto s = __builtin_amdgcn_permlane32_swap(__float_as_uint(x), __float_as_uint(x), false, false);
  return fmaxf(__uint_as_float(s[0]), __uint_as_float(s[1]));
}
__device__ __forceinline__ float tr16(float a, float b) {
  auto s = __builtin_amdgcn_permlane16_swap(__float_as_uint(a), __float_as_uint(b), false, false);
  return __uint_as_float(s[0]) + __uint_as_float(s[1]);
}
__device__ __forceinline__ float tr32(float a, float b) {
  auto s = __builtin_amdgcn_permlane32_swap(__float_as_uint(a), __float_as_uint(b), false, false);
  return __uint_as_float(s[0]) + __uint_as_float(s[1]);
}
__device__ __forceinline__ float wave_sum(float x) {
  x += dpp<XOR1>(x); x += dpp<XOR2>(x); x += dpp<XOR7>(x); x += dpp<XOR8>(x); x = x16_sum(x); return x32_sum(x);
}
__device__ __forceinline__ v4u ldnt(const GAS v4u* p) { return __builtin_nontemporal_load(p); }
__device__ __forceinline__ void st_gran(GAS u64* g, unsigned tag, unsigned val) { __hip_atomic_store(g, ((u64)tag << 32) | val, RLX_AGENT); }
__device__ __forceinline__ u64 ld_gran(GAS u64* g) { return __hip_atomic_load(g, RLX_AGENT); }
__device__ __forceinline__ void lds_barrier() { asm volatile("s_waitcnt lgkmcnt(0)" ::: "memory"); __builtin_amdgcn_s_barrier(); asm volatile("" ::: "memory"); }

struct Params {
  const unsigned short *x, *kc, *vc, *wq, *wo, *w1, *b1, *w2, *b2;
  unsigned short* out;
  unsigned char* ws;
};

constexpr int L_XNP = 0;
constexpr int L_RED = L_XNP + 224 * 8 + 56 * 4;
constexpr int L_Q = L_RED + 2 * 7 * 128;
constexpr int L_MRG = L_Q + 256;
constexpr int L_Y = L_MRG + 8 * 2 * 260;
constexpr int L_S = L_Y + 7 * 448;
constexpr int L_FIN = L_S + 8 * 448;
constexpr int L_END = L_FIN + 256;
constexpr int LDS_BYTES = 96 * 1024;
static_assert(L_END * 4 <= LDS_BYTES, "");
static_assert(L_RED % 4 == 0 && L_Q % 4 == 0 && L_MRG % 4 == 0 && L_Y % 4 == 0 && L_S % 4 == 0 && L_FIN % 4 == 0, "");

constexpr unsigned OOB = 0x80000000u;
__device__ __forceinline__ __amdgpu_buffer_rsrc_t mk_rsrc(const void* p, unsigned bytes) { return __builtin_amdgcn_make_buffer_rsrc((void*)p, 0, bytes, 0x00020000); }
__device__ __forceinline__ void slice_load(__amdgpu_buffer_rsrc_t r, int N, int col0, int tid, bool on, unsigned (&wl)[4], unsigned (&wh)[4]) {
  const int lane = tid & 63, s = (tid >> 6) * 8 + (lane >> 3), cdw = lane & 7;
  const unsigned o0 = (on && tid < 448) ? (unsigned)(2 * s * N + col0) * 2 + cdw * 4 : OOB;
#pragma unroll
  for (int j = 0; j < 4; ++j) {
    wl[j] = __builtin_amdgcn_raw_buffer_load_b32(r, o0 + (unsigned)(112 * j) * N * 2, 0, 0);
    wh[j] = __builtin_amdgcn_raw_buffer_load_b32(r, o0 + (unsigned)(112 * j + 1) * N * 2, 0, 0);
  }
}
__device__ __forceinline__ void slice_dot(const unsigned* lds_u, float* red, int tid, const unsigned (&wl)[4], const unsigned (&wh)[4]) {
  const int lane = tid & 63, wv = tid >> 6, s = wv * 8 + (lane >> 3), cdw = lane & 7;
  float x[16];
#pragma unroll
  for (int v = 0; v < 16; ++v) x[v] = 0.f;
#pragma unroll
  for (int j = 0; j < 4; ++j) {
    const int kp = s + 56 * j;
    const unsigned* xr = lds_u + L_XNP + kp * 8 + (kp >> 2) * 4;
    const v4u xa = *(const v4u*)xr, xb = *(const v4u*)(xr + 4);
    const unsigned pa = __builtin_amdgcn_perm(wh[j], wl[j], 0x05040100u), pb = __builtin_amdgcn_perm(wh[j], wl[j], 0x07060302u);
#pragma unroll
    for (int b = 0; b < 4; ++b) { x[b] = dot2(pa, xa[b], x[b]); x[4 + b] = dot2(pa, xb[b], x[4 + b]); x[8 + b] = dot2(pb, xa[b], x[8 + b]); x[12 + b] = dot2(pb, xb[b], x[12 + b]); }
  }
#pragma unroll
  for (int u = 0; u < 8; ++u) { const float keep = (lane & 8) ? x[u + 8] : x[u], send = (lane & 8) ? x[u] : x[u + 8]; x[u] = keep + dpp<XOR8>(send); }
#pragma unroll
  for (int u = 0; u < 4; ++u) x[u] = tr16(x[u], x[u + 4]);
#pragma unroll
  for (int u = 0; u < 2; ++u) x[u] = tr32(x[u], x[u + 2]);
  const int c = (lane >> 3) & 1, b0 = ((lane >> 4) & 1) * 4 + ((lane >> 5) & 1) * 2;
  *(f32x2*)(red + ((wv * 8 + cdw) * 2 + c) * 8 + b0) = f32x2{x[0], x[1]};
}

__global__ void __launch_bounds__(NTHR, 2) fused_kernel(Params P) {
  extern __shared__ __attribute__((aligned(16))) float lds[];
  unsigned* lds_u = (unsigned*)lds;
  const int tid = threadIdx.x, lane = tid & 63, w = __builtin_amdgcn_readfirstlane(tid >> 6);
  const int bid = blockIdx.x;
  const int kvh = bid & 7, rest = bid >> 3, sp = rest & 3, b = rest >> 2;
  const int pair = b * HKV + kvh;
  const int part = sp * 8 + kvh;
  const int c = lane & 31;
  GAS unsigned char* ws = (GAS unsigned char*)P.ws;
  GAS u64* qg = (GAS u64*)(ws + WS_QG);
  GAS u64* hg = (GAS u64*)(ws + WS_HG);
  GAS u64* pg = (GAS u64*)(ws + WS_PG);
  const __amdgpu_buffer_rsrc_t rs = __builtin_amdgcn_make_buffer_rsrc((void*)P.ws, 0, WS_END, 0x00020000);
  const bool is_ffn = rest < 14;

  {
    const v4u z = {0u, 0u, 0u, 0u};
    if (tid < (int)PG_STRIDE / 2) __builtin_amdgcn_raw_buffer_store_b128(z, rs, WS_PG + (unsigned)(pair * 4 + sp) * (PG_STRIDE * 8) + tid * 16, 0, 16);
    if (tid < 448) __builtin_amdgcn_raw_buffer_store_b128(z, rs, WS_SG + (unsigned)(b * 32 + part) * (448 * 16) + tid * 16, 0, 16);
  }
  const unsigned epoch = __hip_atomic_load((GAS unsigned*)(ws + WS_EPOCH), RLX_AGENT);
  const unsigned l56 = lane < 56 ? lane * 16 : OOB;
  const v4u xv = __builtin_amdgcn_raw_buffer_load_b128(mk_rsrc(P.x, B * D * 2), l56 + w * (D * 2), 0, 0);
  unsigned wl[4], wh[4], fl[4], fh[4];
  slice_load(mk_rsrc(P.wq, D * 4096 * 2), 4096, kvh * 512 + rest * 16, tid, true, wl, wh);
  slice_load(mk_rsrc(P.w1, D * F * 2), F, bid * 16, tid, is_ffn, fl, fh);
  const size_t base = ((size_t)pair * SK + (size_t)sp * KEYS_WG) * DH;
  constexpr int USTR = NW * 64, TSTR = NW * 256;
  const GAS v4u* kp = (const GAS v4u*)(P.kc + base) + w * 64 + lane;
  const GAS v4u* vp = (const GAS v4u*)(P.vc + base) + w * 64 + lane;
  v4u KA[4], VA[4], KB[4], VB[4], Kb[4], Vb[4];
#define LD4(dst, ptr) { _Pragma("unroll") for (int u = 0; u < 4; ++u) dst[u] = ldnt(ptr + u * USTR); ptr += TSTR; __builtin_amdgcn_sched_barrier(0); }
  LD4(KA, kp) LD4(VA, vp)

  {
    float v[8];
#pragma unroll
    for (int j = 0; j < 4; ++j) { v[2 * j] = lo16(xv[j]); v[2 * j + 1] = hi16(xv[j]); }
    float s = 0.f;
#pragma unroll
    for (int j = 0; j < 8; ++j) s += v[j];
    const float mean = wave_sum(s) * (1.f / D);
    float q = 0.f;
#pragma unroll
    for (int j = 0; j < 8; ++j) { const float d = v[j] - mean; q = fmaf(d, d, q); }
    if (lane >= 56) q = 0.f;
    const float var = rbf(wave_sum(q) * (1.f / D));
    const float mb = rbf(mean);
    const float den = rbf(sqrtf(rbf(var + 1e-5f)));
    if (lane < 56) {
#pragma unroll
      for (int j = 0; j < 4; ++j) {
        const unsigned lo = f2bf(rbf(v[2 * j] - mb) / den), hi = f2bf(rbf(v[2 * j + 1] - mb) / den);
        lds_u[L_XNP + (lane * 4 + j) * 8 + lane * 4 + w] = lo | (hi << 16);
      }
    }
  }
  lds_barrier();
  if (tid < 448) slice_dot(lds_u, lds + L_RED, tid, wl, wh);
  LD4(KB, kp) LD4(VB, vp)
  lds_barrier();
  const unsigned T = (epoch + 1u) | 0x80000000u;
  if (w == 0) {
    const int bb = lane >> 3, cdw = lane & 7;
    float v0 = 0.f, v1 = 0.f;
#pragma unroll
    for (int wv = 0; wv < 7; ++wv) { v0 += lds[L_RED + ((wv * 8 + cdw) * 2 + 0) * 8 + bb]; v1 += lds[L_RED + ((wv * 8 + cdw) * 2 + 1) * 8 + bb]; }
    st_gran(qg + (kvh * 8 + bb) * 256 + rest * 8 + cdw, T, f2bf(v0) | (f2bf(v1) << 16));
  }
  if (is_ffn) {
    if (tid < 448) slice_dot(lds_u, lds + L_RED + 896, tid, fl, fh);
    lds_barrier();
    if (w == 1) {
      const int bb = lane >> 3, cdw = lane & 7;
      float v0 = 0.f, v1 = 0.f;
#pragma unroll
      for (int wv = 0; wv < 7; ++wv) { v0 += lds[L_RED + 896 + ((wv * 8 + cdw) * 2 + 0) * 8 + bb]; v1 += lds[L_RED + 896 + ((wv * 8 + cdw) * 2 + 1) * 8 + bb]; }
      const unsigned bw = *(const GAS unsigned*)(P.b1 + bid * 16 + cdw * 2);
      float hv[2] = {rbf(rbf(v0) + lo16(bw)), rbf(rbf(v1) + hi16(bw))};
#pragma unroll
      for (int j = 0; j < 2; ++j) { const float t = hv[j]; const float u = 0.7978845608028654f * (t + 0.044715f * t * t * t);
        const float e = __expf(2.f * u); const float th = 1.f - 2.f / (e + 1.f);
        hv[j] = 0.5f * t * (1.f + th); }
      st_gran(hg + bb * 896 + bid * 8 + cdw, T, f2bf(hv[0]) | (f2bf(hv[1]) << 16));
    }
  }
  if (w == 0) {
    const unsigned off = WS_QG + (unsigned)(kvh * 8 + b) * 2048 + lane * 32;
    v4u g0, g1;
    for (unsigned n = 0; n < SPIN_CAP; ++n) {
      g0 = __builtin_amdgcn_raw_buffer_load_b128(rs, off, 0, 16);
      g1 = __builtin_amdgcn_raw_buffer_load_b128(rs, off + 16, 0, 16);
      if (__all(g0.y == T && g0.w == T && g1.y == T && g1.w == T)) break;
      __builtin_amdgcn_s_sleep(1);
    }
    *(v4u*)(lds_u + L_Q + lane * 4) = v4u{g0.x, g0.z, g1.x, g1.z};
  }
  lds_barrier();
  v4u qv[2];
  qv[0] = *(const v4u*)(lds_u + L_Q + c * 4);
  qv[1] = *(const v4u*)(lds_u + L_Q + 128 + c * 4);

  float acc[2][8];
#pragma unroll
  for (int h = 0; h < 2; ++h)
#pragma unroll
    for (int d = 0; d < 8; ++d) acc[h][d] = 0.f;
  float m = -INFINITY, l = 0.f, p;
  constexpr float C2 = 0.0625f * 1.4426950408889634f;
#define TR_STEP(HALF, CTRL, BIT) _Pragma("unroll") for (int u = 0; u < (HALF); ++u) { \
        const float keep = (lane & (BIT)) ? x[u + (HALF)] : x[u]; const float send = (lane & (BIT)) ? x[u] : x[u + (HALF)]; \
        x[u] = keep + dpp<CTRL>(send); }
#define QKSM(KX) { float x[8]; \
      _Pragma("unroll") for (int h = 0; h < 2; ++h) \
        _Pragma("unroll") for (int i = 0; i < 4; ++i) { float d = 0.f; \
          _Pragma("unroll") for (int k = 0; k < 4; ++k) d = dot2(KX[i][k], qv[h][k], d); \
          x[h * 4 + i] = d; } \
      TR_STEP(4, XOR8, 8) TR_STEP(2, XOR7, 4) TR_STEP(1, XOR2, 2) \
      float s = x[0]; s += dpp<XOR1>(s); s = x16_sum(s); \
      s = rbf(s) * C2; \
      if (__any(s > m)) { \
        float pm = s; pm = fmaxf(pm, dpp<XOR2>(pm)); pm = fmaxf(pm, dpp<XOR7>(pm)); pm = x32_max(pm); \
        const float mn = fmaxf(m, pm); \
        const float alpha = __builtin_amdgcn_exp2f(m - mn); \
        m = mn; l *= alpha; \
        const float a0 = dpp<0x150>(alpha), a1 = dpp<0x158>(alpha); \
        _Pragma("unroll") for (int d = 0; d < 8; ++d) { acc[0][d] *= a0; acc[1][d] *= a1; } \
      } \
      p = __builtin_amdgcn_exp2f(s - m); \
      l += p; \
      __builtin_amdgcn_sched_barrier(0); }
#define PV1(VX, I) { float vf[8]; _Pragma("unroll") for (int k = 0; k < 4; ++k) { vf[2 * k] = lo16(VX[I][k]); vf[2 * k + 1] = hi16(VX[I][k]); } \
        const float p0 = dpp<0x150 + 2 * I>(p), p1 = dpp<0x158 + 2 * I>(p); \
        _Pragma("unroll") for (int d = 0; d < 8; ++d) { acc[0][d] = fmaf(p0, vf[d], acc[0][d]); acc[1][d] = fmaf(p1, vf[d], acc[1][d]); } }
#define PV(VX) { PV1(VX, 0) PV1(VX, 1) PV1(VX, 2) PV1(VX, 3) __builtin_amdgcn_sched_barrier(0); }
#define SEG(N) _Pragma("unroll 1") for (int t = 0; t < (N); ++t) { LD4(Kb, kp) QKSM(Kb) LD4(Vb, vp) PV(Vb) }
  LD4(Kb, kp)
  QKSM(KA) PV(VA) QKSM(KB) PV(VB)
  QKSM(Kb) LD4(Vb, vp) PV(Vb)
  SEG(3)
  {
    v4u w2r[8]; u64 rg[4];
    {
      const __amdgpu_buffer_rsrc_t r2 = mk_rsrc(P.w2, F * D * 2);
      const unsigned o2 = w < 7 ? l56 + (unsigned)(56 * part + 8 * w) * (D * 2) : OOB;
#pragma unroll
      for (int r = 0; r < 8; ++r) w2r[r] = __builtin_amdgcn_raw_buffer_load_b128(r2, o2 + r * (D * 2), 0, 0);
    }
    GAS u64* rgp[4];
#pragma unroll
    for (int j = 0; j < 4; ++j) { const int i = lane * 4 + j; rgp[j] = w < 7 ? hg + b * 896 + 28 * part + 4 * w + (lane & 3) : qg + ((i & 7) * 8) * 256 + (i >> 3) * 8; rg[j] = ld_gran(rgp[j]); }
    SEG(1)
    for (unsigned n = 0; n < SPIN_CAP; ++n) {
      if (__all((unsigned)(rg[0] >> 32) == T && (unsigned)(rg[1] >> 32) == T && (unsigned)(rg[2] >> 32) == T && (unsigned)(rg[3] >> 32) == T)) break;
      __builtin_amdgcn_s_sleep(1);
#pragma unroll
      for (int j = 0; j < 4; ++j) rg[j] = ld_gran(rgp[j]);
    }
    if (w < 7) {
      float ya[8];
#pragma unroll
      for (int j = 0; j < 8; ++j) ya[j] = 0.f;
#pragma unroll
      for (int i = 0; i < 4; ++i) {
        const unsigned hp = __builtin_amdgcn_readlane((unsigned)rg[0], i);
#pragma unroll
        for (int q = 0; q < 4; ++q) {
          ya[2 * q] = dot2(__builtin_amdgcn_perm(w2r[2 * i + 1][q], w2r[2 * i][q], 0x05040100u), hp, ya[2 * q]);
          ya[2 * q + 1] = dot2(__builtin_amdgcn_perm(w2r[2 * i + 1][q], w2r[2 * i][q], 0x07060302u), hp, ya[2 * q + 1]);
        }
      }
      if (lane < 56) { *(f32x4*)(lds + L_Y + w * 448 + lane * 8) = f32x4{ya[0], ya[1], ya[2], ya[3]}; *(f32x4*)(lds + L_Y + w * 448 + lane * 8 + 4) = f32x4{ya[4], ya[5], ya[6], ya[7]}; }
    } else {
      if (bid == 0 && lane == 0) __hip_atomic_store((GAS unsigned*)(ws + WS_EPOCH), epoch + 1u, RLX_AGENT);
    }
  }
  SEG(9)
  v4u wor[16];
  {
    const int k0 = (kvh * 2 + (sp >> 1)) * 256 + (sp & 1) * 128 + 16 * w;
    const __amdgpu_buffer_rsrc_t ro = mk_rsrc(P.wo, 4096 * D * 2);
#pragma unroll
    for (int r = 0; r < 16; ++r) wor[r] = __builtin_amdgcn_raw_buffer_load_b128(ro, l56 + (unsigned)(k0 + r) * (D * 2), 0, 0);
  }
  SEG(2)
#pragma unroll
  for (int i = 0; i < 8; ++i)
#pragma unroll
    for (int q = 0; q < 4; ++q) {
      const unsigned a = wor[2 * i][q], bq = wor[2 * i + 1][q];
      wor[2 * i][q] = __builtin_amdgcn_perm(bq, a, 0x05040100u);
      wor[2 * i + 1][q] = __builtin_amdgcn_perm(bq, a, 0x07060302u);
    }
  float xin = 0.f, b2v = 0.f;
  if (part < 28 && tid < 16) { xin = bf2f(P.x[b * D + part * 16 + tid]); b2v = bf2f(P.b2[part * 16 + tid]); }
  SEG(14)

#pragma unroll
  for (int h = 0; h < 2; ++h)
#pragma unroll
    for (int d = 0; d < 8; ++d) acc[h][d] = x32_sum(acc[h][d]);
  l += dpp<XOR2>(l); l += dpp<XOR7>(l); l = x32_sum(l);
  {
    float* mrg = lds + L_MRG + w * 520;
    if (lane < 32) {
#pragma unroll
      for (int h = 0; h < 2; ++h) {
        *(f32x4*)(mrg + h * 260 + 4 + c * 8) = f32x4{acc[h][0], acc[h][1], acc[h][2], acc[h][3]};
        *(f32x4*)(mrg + h * 260 + 4 + c * 8 + 4) = f32x4{acc[h][4], acc[h][5], acc[h][6], acc[h][7]};
      }
      if ((lane & 7) == 0 && lane < 16) { mrg[(lane >> 3) * 260] = m; mrg[(lane >> 3) * 260 + 1] = l; }
    }
  }
  lds_barrier();
  {
    const int h = tid >> 8, d = tid & 255;
    const float* mrg = lds + L_MRG + h * 260;
    float M = -INFINITY;
#pragma unroll
    for (int ww = 0; ww < NW; ++ww) M = fmaxf(M, mrg[ww * 520]);
    float L = 0.f, o = 0.f;
#pragma unroll
    for (int ww = 0; ww < NW; ++ww) { const float wt = __builtin_amdgcn_exp2f(mrg[ww * 520] - M); L = fmaf(wt, mrg[ww * 520 + 1], L); o = fmaf(wt, mrg[ww * 520 + 4 + d], o); }
    GAS u64* slot = pg + (size_t)(pair * 4 + sp) * PG_STRIDE;
    st_gran(slot + h * 256 + d, T, __float_as_uint(o));
    if (d < 2) st_gran(slot + 512 + 2 * h + d, T, __float_as_uint(d == 0 ? M : L));
  }
  {
    const int hs = sp >> 1, pj = lane >> 4, j = lane & 15;
    GAS u64* slot = pg + (size_t)(pair * 4 + pj) * PG_STRIDE;
    GAS u64* ga = slot + hs * 256 + (sp & 1) * 128 + 16 * w + j;
    GAS u64* gm = slot + 512 + 2 * hs + (j & 1);
    u64 a, ml;
    for (unsigned n = 0; n < SPIN_CAP; ++n) {
      a = ld_gran(ga); ml = ld_gran(gm);
      if (__all((unsigned)(a >> 32) == T && (unsigned)(ml >> 32) == T)) break;
      __builtin_amdgcn_s_sleep(1);
    }
    const float mlv = __uint_as_float((unsigned)ml), oth = dpp<XOR1>(mlv);
    const float Mp = (j & 1) ? oth : mlv, Lp = (j & 1) ? mlv : oth;
    const float M = x32_max(x16_max(Mp));
    const float wt = __builtin_amdgcn_exp2f(Mp - M);
    const float L = x32_sum(x16_sum(wt * Lp));
    const float o = x32_sum(x16_sum(wt * __uint_as_float((unsigned)a)));
    const unsigned ob = f2bf(o / L);
    const unsigned pk = ob | (__builtin_bit_cast(unsigned, dpp<XOR1>(__builtin_bit_cast(float, ob))) << 16);
    float sa[8];
#pragma unroll
    for (int q = 0; q < 8; ++q) sa[q] = 0.f;
#pragma unroll
    for (int i = 0; i < 8; ++i) {
      const unsigned op = __builtin_amdgcn_readlane(pk, 2 * i);
#pragma unroll
      for (int q = 0; q < 4; ++q) { sa[2 * q] = dot2(wor[2 * i][q], op, sa[2 * q]); sa[2 * q + 1] = dot2(wor[2 * i + 1][q], op, sa[2 * q + 1]); }
    }
    if (lane < 56) { *(f32x4*)(lds + L_S + w * 448 + lane * 8) = f32x4{sa[0], sa[1], sa[2], sa[3]}; *(f32x4*)(lds + L_S + w * 448 + lane * 8 + 4) = f32x4{sa[4], sa[5], sa[6], sa[7]}; }
  }
  lds_barrier();
  if (tid < 448) {
    float s = 0.f, y = 0.f;
#pragma unroll
    for (int ww = 0; ww < 8; ++ww) s += lds[L_S + ww * 448 + tid];
#pragma unroll
    for (int ww = 0; ww < 7; ++ww) y += lds[L_Y + ww * 448 + tid];
    __builtin_amdgcn_raw_buffer_store_b128(v4u{__float_as_uint(s), T, __float_as_uint(y), T}, rs, WS_SG + (unsigned)(b * 32 + part) * (448 * 16) + tid * 16, 0, 16);
  }
  if (part < 28) {
    const int pp = tid >> 4, col = tid & 15;
    const unsigned off = WS_SG + (unsigned)(b * 32 + pp) * (448 * 16) + (part * 16 + col) * 16;
    v4u g;
    for (unsigned n = 0; n < SPIN_CAP; ++n) {
      g = __builtin_amdgcn_raw_buffer_load_b128(rs, off, 0, 16);
      if (__all(g.y == T && g.w == T)) break;
      __builtin_amdgcn_s_sleep(1);
    }
    const float s = x32_sum(x16_sum(__uint_as_float(g.x))), y = x32_sum(x16_sum(__uint_as_float(g.z)));
    if (lane < 16) *(f32x2*)(lds + L_FIN + (w * 16 + lane) * 2) = f32x2{s, y};
    lds_barrier();
    if (tid < 16) {
      float st = 0.f, yt = 0.f;
#pragma unroll
      for (int ww = 0; ww < 8; ++ww) { st += lds[L_FIN + (ww * 16 + tid) * 2]; yt += lds[L_FIN + (ww * 16 + tid) * 2 + 1]; }
      st = rbf(st);
      yt = rbf(rbf(yt) + b2v);
      const float g2 = rbf(yt / (1.f + __expf(-yt)));
      P.out[b * D + part * 16 + tid] = (unsigned short)f2bf(xin + rbf(st * g2));
    }
  }
}

extern "C" void kernel_launch(void* const* d_in, const int* in_sizes, int n_in, void* d_out, int out_size, void* d_ws, size_t ws_size,
                              hipStream_t stream) {
  Params P;
  P.x = (const unsigned short*)d_in[0];
  P.kc = (const unsigned short*)d_in[1];
  P.vc = (const unsigned short*)d_in[2];
  P.wq = (const unsigned short*)d_in[3];
  P.wo = (const unsigned short*)d_in[4];
  P.w1 = (const unsigned short*)d_in[6];
  P.b1 = (const unsigned short*)d_in[7];
  P.w2 = (const unsigned short*)d_in[8];
  P.b2 = (const unsigned short*)d_in[9];
  P.out = (unsigned short*)d_out;
  P.ws = (unsigned char*)d_ws;
  static bool attr_set = false;
  if (!attr_set) { (void)hipFuncSetAttribute((const void*)fused_kernel, hipFuncAttributeMaxDynamicSharedMemorySize, LDS_BYTES); attr_set = true; }
  fused_kernel<<<NWG, NTHR, LDS_BYTES, stream>>>(P);
}
```

```cpp
#include <hip/hip_runtime.h>
#include <hip/hip_bf16.h>
#include <stdint.h>

typedef unsigned v4u __attribute__((ext_vector_type(4)));
typedef unsigned v2u __attribute__((ext_vector_type(2)));
typedef float f32x4 __attribute__((ext_vector_type(4)));
typedef float f32x2 __attribute__((ext_vector_type(2)));
typedef __attribute__((ext_vector_type(2))) __bf16 bf16x2;
typedef unsigned long long u64;
#define GAS __attribute__((address_space(1)))
#define RLX_AGENT __ATOMIC_RELAXED, __HIP_MEMORY_SCOPE_AGENT

constexpr int B = 8, D = 448, HKV = 8, SK = 8192, DH = 256, F = 1792;
constexpr int NWG = 256, NW = 8, NTHR = 512;
#define GS 24
constexpr int NDYN = (128 - 4 * GS) * 8;
constexpr unsigned SPIN_CAP = 1u << 22;

constexpr unsigned WS_EPOCH = 0;
constexpr unsigned WS_CTR = 4096;
constexpr unsigned WS_QG = WS_CTR + 64 * 256;
constexpr unsigned WS_HG = WS_QG + 8 * 8 * 256 * 8;
constexpr unsigned WS_PG = WS_HG + 8 * 896 * 8;
constexpr unsigned PG_STRIDE = 576;
constexpr unsigned WS_SG = WS_PG + 256 * PG_STRIDE * 8;
constexpr unsigned WS_END = WS_SG + 8 * 32 * 448 * 16;

__device__ __forceinline__ float bf2f(unsigned short b) { return __builtin_bit_cast(float, (unsigned)b << 16); }
__device__ __forceinline__ float rbf(float f) { return (float)(__bf16)f; }
__device__ __forceinline__ unsigned f2bf(float f) { return (unsigned)__builtin_bit_cast(unsigned short, (__bf16)f); }
__device__ __forceinline__ float lo16(unsigned u) { return __builtin_bit_cast(float, u << 16); }
__device__ __forceinline__ float hi16(unsigned u) { return __builtin_bit_cast(float, u & 0xffff0000u); }
__device__ __forceinline__ float dot2(unsigned k, unsigned q, float acc) {
  return __builtin_amdgcn_fdot2_f32_bf16(__builtin_bit_cast(bf16x2, k), __builtin_bit_cast(bf16x2, q), acc, false);
}
template <int CTRL> __device__ __forceinline__ float dpp(float x) {
  return __builtin_bit_cast(float, __builtin_amdgcn_mov_dpp(__builtin_bit_cast(int, x), CTRL, 0xf, 0xf, true));
}
constexpr int XOR1 = 0xB1, XOR2 = 0x4E, XOR7 = 0x141, XOR8 = 0x128;
__device__ __forceinline__ float x16_sum(float x) {
  auto s = __builtin_amdgcn_permlane16_swap(__float_as_uint(x), __float_as_uint(x), false, false);
  return __uint_as_float(s[0]) + __uint_as_float(s[1]);
}
__device__ __forceinline__ float x32_sum(float x) {
  auto s = __builtin_amdgcn_permlane32_swap(__float_as_uint(x), __float_as_uint(x), false, false);
  return __uint_as_float(s[0]) + __uint_as_float(s[1]);
}
__device__ __forceinline__ float x16_max(float x) {
  auto s = __builtin_amdgcn_permlane16_swap(__float_as_uint(x), __float_as_uint(x), false, false);
  return fmaxf(__uint_as_float(s[0]), __uint_as_float(s[1]));
}
__device__ __forceinline__ float x32_max(float x) {
  auto s = __builtin_amdgcn_permlane32_swap(__float_as_uint(x), __float_as_uint(x), false, false);
  return fmaxf(__uint_as_float(s[0]), __uint_as_float(s[1]));
}
__device__ __forceinline__ float tr16(float a, float b) {
  auto s = __builtin_amdgcn_permlane16_swap(__float_as_uint(a), __float_as_uint(b), false, false);
  return __uint_as_float(s[0]) + __uint_as_float(s[1]);
}
__device__ __forceinline__ float tr32(float a, float b) {
  auto s = __builtin_amdgcn_permlane32_swap(__float_as_uint(a), __float_as_uint(b), false, false);
  return __uint_as_float(s[0]) + __uint_as_float(s[1]);
}
__device__ __forceinline__ float wave_sum(float x) {
  x += dpp<XOR1>(x); x += dpp<XOR2>(x); x += dpp<XOR7>(x); x += dpp<XOR8>(x); x = x16_sum(x); return x32_sum(x);
}
__device__ __forceinline__ v4u ldnt(const GAS v4u* p) { return __builtin_nontemporal_load(p); }
__device__ __forceinline__ void st_gran(GAS u64* g, unsigned tag, unsigned val) { __hip_atomic_store(g, ((u64)tag << 32) | val, RLX_AGENT); }
__device__ __forceinline__ u64 ld_gran(GAS u64* g) { return __hip_atomic_load(g, RLX_AGENT); }
__device__ __forceinline__ void lds_barrier() { asm volatile("s_waitcnt lgkmcnt(0)" ::: "memory"); __builtin_amdgcn_s_barrier(); asm volatile("" ::: "memory"); }

#define STAMP(i) do { } while (0)
struct Params {
  const unsigned short *x, *kc, *vc, *wq, *wo, *w1, *b1, *w2, *b2;
  unsigned short* out;
  unsigned char* ws;
};

constexpr int L_XNP = 0;
constexpr int L_RED = L_XNP + 224 * 8 + 56 * 4;
constexpr int L_Q = L_RED + 2 * 7 * 128;
constexpr int L_MRG = L_Q + 256;
constexpr int L_Y = L_MRG + 8 * 2 * 260;
constexpr int L_S = L_Y + 7 * 448;
constexpr int L_FIN = L_S + 8 * 448;
constexpr int L_END = L_FIN + 256;
constexpr int LDS_BYTES = 96 * 1024;
static_assert(L_END * 4 <= LDS_BYTES, "");
static_assert(L_RED % 4 == 0 && L_Q % 4 == 0 && L_MRG % 4 == 0 && L_Y % 4 == 0 && L_S % 4 == 0 && L_FIN % 4 == 0, "");

constexpr unsigned OOB = 0x80000000u;
__device__ __forceinline__ __amdgpu_buffer_rsrc_t mk_rsrc(const void* p, unsigned bytes) { return __builtin_amdgcn_make_buffer_rsrc((void*)p, 0, bytes, 0x00020000); }
__device__ __forceinline__ void slice_load(__amdgpu_buffer_rsrc_t r, int N, int col0, int tid, bool on, unsigned (&wl)[4], unsigned (&wh)[4]) {
  const int lane = tid & 63, s = (tid >> 6) * 8 + (lane >> 3), cdw = lane & 7;
  const unsigned o0 = (on && tid < 448) ? (unsigned)(2 * s * N + col0) * 2 + cdw * 4 : OOB;
#pragma unroll
  for (int j = 0; j < 4; ++j) {
    wl[j] = __builtin_amdgcn_raw_buffer_load_b32(r, o0 + (unsigned)(112 * j) * N * 2, 0, 0);
    wh[j] = __builtin_amdgcn_raw_buffer_load_b32(r, o0 + (unsigned)(112 * j + 1) * N * 2, 0, 0);
  }
}
__device__ __forceinline__ void slice_dot(const unsigned* lds_u, float* red, int tid, const unsigned (&wl)[4], const unsigned (&wh)[4]) {
  const int lane = tid & 63, wv = tid >> 6, s = wv * 8 + (lane >> 3), cdw = lane & 7;
  float x[16];
#pragma unroll
  for (int v = 0; v < 16; ++v) x[v] = 0.f;
#pragma unroll
  for (int j = 0; j < 4; ++j) {
    const int kp = s + 56 * j;
    const unsigned* xr = lds_u + L_XNP + kp * 8 + (kp >> 2) * 4;
    const v4u xa = *(const v4u*)xr, xb = *(const v4u*)(xr + 4);
    const unsigned pa = __builtin_amdgcn_perm(wh[j], wl[j], 0x05040100u), pb = __builtin_amdgcn_perm(wh[j], wl[j], 0x07060302u);
#pragma unroll
    for (int b = 0; b < 4; ++b) { x[b] = dot2(pa, xa[b], x[b]); x[4 + b] = dot2(pa, xb[b], x[4 + b]); x[8 + b] = dot2(pb, xa[b], x[8 + b]); x[12 + b] = dot2(pb, xb[b], x[12 + b]); }
  }
#pragma unroll
  for (int u = 0; u < 8; ++u) { const float keep = (lane & 8) ? x[u + 8] : x[u], send = (lane & 8) ? x[u] : x[u + 8]; x[u] = keep + dpp<XOR8>(send); }
#pragma unroll
  for (int u = 0; u < 4; ++u) x[u] = tr16(x[u], x[u + 4]);
#pragma unroll
  for (int u = 0; u < 2; ++u) x[u] = tr32(x[u], x[u + 2]);
  const int c = (lane >> 3) & 1, b0 = ((lane >> 4) & 1) * 4 + ((lane >> 5) & 1) * 2;
  *(f32x2*)(red + ((wv * 8 + cdw) * 2 + c) * 8 + b0) = f32x2{x[0], x[1]};
}

__global__ void __launch_bounds__(NTHR, 2) fused_kernel(Params P) {
  extern __shared__ __attribute__((aligned(16))) float lds[];
  unsigned* lds_u = (unsigned*)lds;
  const int tid = threadIdx.x, lane = tid & 63, w = __builtin_amdgcn_readfirstlane(tid >> 6);
  const int bid = blockIdx.x;
  const int pair = bid >> 2, sp = bid & 3, b = pair >> 3, kvh = pair & 7;
  const int rest = b * 4 + sp;
  const int part = kvh * 4 + sp;
  const int c = lane & 31;
  GAS unsigned char* ws = (GAS unsigned char*)P.ws;
  GAS u64* qg = (GAS u64*)(ws + WS_QG);
  GAS u64* hg = (GAS u64*)(ws + WS_HG);
  GAS u64* pg = (GAS u64*)(ws + WS_PG);
  const __amdgpu_buffer_rsrc_t rs = __builtin_amdgcn_make_buffer_rsrc((void*)P.ws, 0, WS_END, 0x00020000);
  const bool is_ffn = bid < 112;

  STAMP(0);
  const unsigned epoch = __hip_atomic_load((GAS unsigned*)(ws + WS_EPOCH), RLX_AGENT);
  const unsigned l56 = lane < 56 ? lane * 16 : OOB;
  const v4u xv = __builtin_amdgcn_raw_buffer_load_b128(mk_rsrc(P.x, B * D * 2), l56 + w * (D * 2), 0, 0);
  unsigned wl[4], wh[4], fl[4], fh[4];
  slice_load(mk_rsrc(P.wq, D * 4096 * 2), 4096, kvh * 512 + rest * 16, tid, true, wl, wh);
  slice_load(mk_rsrc(P.w1, D * F * 2), F, bid * 16, tid, is_ffn, fl, fh);
  const unsigned b1w = __builtin_amdgcn_raw_buffer_load_b32(mk_rsrc(P.b1, F * 2), is_ffn ? (unsigned)(bid * 16 + (lane & 7) * 2) * 2 : OOB, 0, 0);
  const size_t base = (size_t)pair * SK * DH;
  constexpr int USTR = NW * 64, TSTR = NW * 256;
  const GAS v4u* kbase = (const GAS v4u*)(P.kc + base) + lane;
  const GAS v4u* vbase = (const GAS v4u*)(P.vc + base) + lane;
  const GAS v4u* kp = kbase + (size_t)sp * GS * TSTR + w * 64;
  const GAS v4u* vp = vbase + (size_t)sp * GS * TSTR + w * 64;
  v4u KA[4], VA[4], KB[4], VB[4], KC[4], VC[4], Kb[4], Vb[4];
#define LD4(dst, ptr) { _Pragma("unroll") for (int u = 0; u < 4; ++u) dst[u] = ldnt(ptr + u * USTR); ptr += TSTR; __builtin_amdgcn_sched_barrier(0); }
#define DRAIN() asm volatile("s_waitcnt vmcnt(0)" ::: "memory")
  LD4(KA, kp)
  {
    const v4u z = {0u, 0u, 0u, 0u};
    if (tid < (int)PG_STRIDE / 2) __builtin_amdgcn_raw_buffer_store_b128(z, rs, WS_PG + (unsigned)(pair * 4 + sp) * (PG_STRIDE * 8) + tid * 16, 0, 16);
    if (tid < 448) __builtin_amdgcn_raw_buffer_store_b128(z, rs, WS_SG + (unsigned)(b * 32 + part) * (448 * 16) + tid * 16, 0, 16);
    if (sp == 0 && tid == 511) __builtin_amdgcn_raw_buffer_store_b32(0u, rs, WS_CTR + pair * 256, 0, 16);
  }
  __builtin_amdgcn_sched_barrier(0);
  {
    float v[8];
#pragma unroll
    for (int j = 0; j < 4; ++j) { v[2 * j] = lo16(xv[j]); v[2 * j + 1] = hi16(xv[j]); }
    asm volatile("" : "+v"(v[0]), "+v"(v[1]), "+v"(v[2]), "+v"(v[3]), "+v"(v[4]), "+v"(v[5]), "+v"(v[6]), "+v"(v[7]));
    STAMP(10);
    LD4(VA, vp)
    float s = 0.f;
#pragma unroll
    for (int j = 0; j < 8; ++j) s += v[j];
    const float mean = wave_sum(s) * (1.f / D);
    float q = 0.f;
#pragma unroll
    for (int j = 0; j < 8; ++j) { const float d = v[j] - mean; q = fmaf(d, d, q); }
    if (lane >= 56) q = 0.f;
    const float var = rbf(wave_sum(q) * (1.f / D));
    const float mb = rbf(mean);
    const float inv = 1.f / rbf(sqrtf(rbf(var + 1e-5f)));
    if (lane < 56) {
#pragma unroll
      for (int j = 0; j < 4; ++j) {
        const unsigned lo = f2bf(rbf(v[2 * j] - mb) * inv), hi = f2bf(rbf(v[2 * j + 1] - mb) * inv);
        lds_u[L_XNP + (lane * 4 + j) * 8 + lane * 4 + w] = lo | (hi << 16);
      }
    }
  }
  lds_barrier();
  STAMP(1);
  if (tid < 448) slice_dot(lds_u, lds + L_RED, tid, wl, wh);
  STAMP(11);
  asm volatile("s_waitcnt vmcnt(4)" ::: "memory");
  LD4(KB, kp)
  lds_barrier();
  STAMP(12);
  const unsigned T = (epoch + 1u) | 0x80000000u;
  if (w == 0) {
    const int bb = lane >> 3, cdw = lane & 7;
    float v0 = 0.f, v1 = 0.f;
#pragma unroll
    for (int wv = 0; wv < 7; ++wv) { v0 += lds[L_RED + ((wv * 8 + cdw) * 2 + 0) * 8 + bb]; v1 += lds[L_RED + ((wv * 8 + cdw) * 2 + 1) * 8 + bb]; }
    st_gran(qg + (kvh * 8 + bb) * 256 + rest * 8 + cdw, T, f2bf(v0) | (f2bf(v1) << 16));
  }
  STAMP(2);
  if (is_ffn) {
    if (tid < 448) slice_dot(lds_u, lds + L_RED + 896, tid, fl, fh);
    lds_barrier();
    if (w == 1) {
      const int bb = lane >> 3, cdw = lane & 7;
      float v0 = 0.f, v1 = 0.f;
#pragma unroll
      for (int wv = 0; wv < 7; ++wv) { v0 += lds[L_RED + 896 + ((wv * 8 + cdw) * 2 + 0) * 8 + bb]; v1 += lds[L_RED + 896 + ((wv * 8 + cdw) * 2 + 1) * 8 + bb]; }
      float hv[2] = {rbf(rbf(v0) + lo16(b1w)), rbf(rbf(v1) + hi16(b1w))};
#pragma unroll
      for (int j = 0; j < 2; ++j) { const float t = hv[j]; const float u = 0.7978845608028654f * (t + 0.044715f * t * t * t);
        const float e = __expf(2.f * u); const float th = 1.f - 2.f / (e + 1.f);
        hv[j] = 0.5f * t * (1.f + th); }
      st_gran(hg + bb * 896 + bid * 8 + cdw, T, f2bf(hv[0]) | (f2bf(hv[1]) << 16));
    }
  }
  if (w == 0) {
    LD4(VB, vp)
    const unsigned off = WS_QG + (unsigned)(kvh * 8 + b) * 2048 + lane * 32;
    v4u g0, g1;
    for (unsigned n = 0; n < SPIN_CAP; ++n) {
      g0 = __builtin_amdgcn_raw_buffer_load_b128(rs, off, 0, 16);
      g1 = __builtin_amdgcn_raw_buffer_load_b128(rs, off + 16, 0, 16);
      if (__all(g0.y == T && g0.w == T && g1.y == T && g1.w == T)) break;
      __builtin_amdgcn_s_sleep(1);
    }
    *(v4u*)(lds_u + L_Q + lane * 4) = v4u{g0.x, g0.z, g1.x, g1.z};
    LD4(KC, kp) LD4(VC, vp)
  } else {
    DRAIN(); LD4(VB, vp)
    DRAIN(); LD4(KC, kp)
    DRAIN(); LD4(VC, vp)
  }
  lds_barrier();
  STAMP(3);
  v4u qv[2];
  qv[0] = *(const v4u*)(lds_u + L_Q + c * 4);
  qv[1] = *(const v4u*)(lds_u + L_Q + 128 + c * 4);

  float acc[2][8];
#pragma unroll
  for (int h = 0; h < 2; ++h)
#pragma unroll
    for (int d = 0; d < 8; ++d) acc[h][d] = 0.f;
  float m = -INFINITY, l = 0.f, p;
  constexpr float C2 = 0.0625f * 1.4426950408889634f;
#define TR_STEP(HALF, CTRL, BIT) _Pragma("unroll") for (int u = 0; u < (HALF); ++u) { \
        const float keep = (lane & (BIT)) ? x[u + (HALF)] : x[u]; const float send = (lane & (BIT)) ? x[u] : x[u + (HALF)]; \
        x[u] = keep + dpp<CTRL>(send); }
#define QKSM(KX) { float x[8]; \
      _Pragma("unroll") for (int h = 0; h < 2; ++h) \
        _Pragma("unroll") for (int i = 0; i < 4; ++i) { float d = 0.f; \
          _Pragma("unroll") for (int k = 0; k < 4; ++k) d = dot2(KX[i][k], qv[h][k], d); \
          x[h * 4 + i] = d; } \
      TR_STEP(4, XOR8, 8) TR_STEP(2, XOR7, 4) TR_STEP(1, XOR2, 2) \
      float s = x[0]; s += dpp<XOR1>(s); s = x16_sum(s); \
      s = rbf(s) * C2; \
      if (__any(s > m)) { \
        float pm = s; pm = fmaxf(pm, dpp<XOR2>(pm)); pm = fmaxf(pm, dpp<XOR7>(pm)); pm = x32_max(pm); \
        const float mn = fmaxf(m, pm); \
        const float alpha = __builtin_amdgcn_exp2f(m - mn); \
        m = mn; l *= alpha; \
        const float a0 = dpp<0x150>(alpha), a1 = dpp<0x158>(alpha); \
        _Pragma("unroll") for (int d = 0; d < 8; ++d) { acc[0][d] *= a0; acc[1][d] *= a1; } \
      } \
      p = __builtin_amdgcn_exp2f(s - m); \
      l += p; \
      __builtin_amdgcn_sched_barrier(0); }
#define PV1(VX, I) { float vf[8]; _Pragma("unroll") for (int k = 0; k < 4; ++k) { vf[2 * k] = lo16(VX[I][k]); vf[2 * k + 1] = hi16(VX[I][k]); } \
        const float p0 = dpp<0x150 + 2 * I>(p), p1 = dpp<0x158 + 2 * I>(p); \
        _Pragma("unroll") for (int d = 0; d < 8; ++d) { acc[0][d] = fmaf(p0, vf[d], acc[0][d]); acc[1][d] = fmaf(p1, vf[d], acc[1][d]); } }
#define PV(VX) { PV1(VX, 0) PV1(VX, 1) PV1(VX, 2) PV1(VX, 3) __builtin_amdgcn_sched_barrier(0); }
#define SEG(N) _Pragma("unroll 1") for (int t = 0; t < (N); ++t) { LD4(Kb, kp) QKSM(Kb) LD4(Vb, vp) PV(Vb) }
  LD4(Kb, kp)
  QKSM(KA) PV(VA) QKSM(KB) PV(VB) QKSM(KC) PV(VC)
  QKSM(Kb) LD4(Vb, vp) PV(Vb)
  SEG(2)
  {
    v4u w2r[8]; u64 rg[4];
    {
      const __amdgpu_buffer_rsrc_t r2 = mk_rsrc(P.w2, F * D * 2);
      const unsigned o2 = w < 7 ? l56 + (unsigned)(56 * part + 8 * w) * (D * 2) : OOB;
#pragma unroll
      for (int r = 0; r < 8; ++r) w2r[r] = __builtin_amdgcn_raw_buffer_load_b128(r2, o2 + r * (D * 2), 0, 0);
    }
    GAS u64* rgp[4];
#pragma unroll
    for (int j = 0; j < 4; ++j) { const int i = lane * 4 + j; rgp[j] = w < 7 ? hg + b * 896 + 28 * part + 4 * w + (lane & 3) : qg + ((i & 7) * 8) * 256 + (i >> 3) * 8; rg[j] = ld_gran(rgp[j]); }
    SEG(1)
    for (unsigned n = 0; n < SPIN_CAP; ++n) {
      if (__all((unsigned)(rg[0] >> 32) == T && (unsigned)(rg[1] >> 32) == T && (unsigned)(rg[2] >> 32) == T && (unsigned)(rg[3] >> 32) == T)) break;
      __builtin_amdgcn_s_sleep(1);
#pragma unroll
      for (int j = 0; j < 4; ++j) rg[j] = ld_gran(rgp[j]);
    }
    if (w < 7) {
      float ya[8];
#pragma unroll
      for (int j = 0; j < 8; ++j) ya[j] = 0.f;
#pragma unroll
      for (int i = 0; i < 4; ++i) {
        const unsigned hp = __builtin_amdgcn_readlane((unsigned)rg[0], i);
#pragma unroll
        for (int q = 0; q < 4; ++q) {
          ya[2 * q] = dot2(__builtin_amdgcn_perm(w2r[2 * i + 1][q], w2r[2 * i][q], 0x05040100u), hp, ya[2 * q]);
          ya[2 * q + 1] = dot2(__builtin_amdgcn_perm(w2r[2 * i + 1][q], w2r[2 * i][q], 0x07060302u), hp, ya[2 * q + 1]);
        }
      }
      if (lane < 56) { *(f32x4*)(lds + L_Y + w * 448 + lane * 8) = f32x4{ya[0], ya[1], ya[2], ya[3]}; *(f32x4*)(lds + L_Y + w * 448 + lane * 8 + 4) = f32x4{ya[4], ya[5], ya[6], ya[7]}; }
    } else {
      if (bid == 0 && lane == 0) __hip_atomic_store((GAS unsigned*)(ws + WS_EPOCH), epoch + 1u, RLX_AGENT);
    }
  }
  SEG(9)
  v4u wor[16];
  {
    const int k0 = (kvh * 2 + (sp >> 1)) * 256 + (sp & 1) * 128 + 16 * w;
    const __amdgpu_buffer_rsrc_t ro = mk_rsrc(P.wo, 4096 * D * 2);
#pragma unroll
    for (int r = 0; r < 16; ++r) wor[r] = __builtin_amdgcn_raw_buffer_load_b128(ro, l56 + (unsigned)(k0 + r) * (D * 2), 0, 0);
  }
  SEG(2)
#pragma unroll
  for (int i = 0; i < 8; ++i)
#pragma unroll
    for (int q = 0; q < 4; ++q) {
      const unsigned a = wor[2 * i][q], bq = wor[2 * i + 1][q];
      wor[2 * i][q] = __builtin_amdgcn_perm(bq, a, 0x05040100u);
      wor[2 * i + 1][q] = __builtin_amdgcn_perm(bq, a, 0x07060302u);
    }
  float xin = 0.f, b2v = 0.f;
  if (part < 28 && tid < 16) { xin = bf2f(P.x[b * D + part * 16 + tid]); b2v = bf2f(P.b2[part * 16 + tid]); }
  SEG(GS - 19)
  {
    GAS unsigned* ctr = (GAS unsigned*)(ws + WS_CTR + pair * 256);
    unsigned nx = 0;
    if (lane == 0) nx = __hip_atomic_fetch_add(ctr, 1u, RLX_AGENT);
    SEG(1)
    unsigned cur = __builtin_amdgcn_readfirstlane(nx);
#pragma unroll 1
    while (cur < (unsigned)NDYN) {
      if (lane == 0) nx = __hip_atomic_fetch_add(ctr, 1u, RLX_AGENT);
      const size_t toff = (size_t)(4 * GS + (cur >> 3)) * TSTR + (cur & 7) * 64;
      kp = kbase + toff; vp = vbase + toff;
      LD4(Kb, kp) QKSM(Kb) LD4(Vb, vp) PV(Vb)
      cur = __builtin_amdgcn_readfirstlane(nx);
    }
  }

  STAMP(4);
#pragma unroll
  for (int h = 0; h < 2; ++h)
#pragma unroll
    for (int d = 0; d < 8; ++d) acc[h][d] = x32_sum(acc[h][d]);
  l += dpp<XOR2>(l); l += dpp<XOR7>(l); l = x32_sum(l);
  {
    float* mrg = lds + L_MRG + w * 520;
    if (lane < 32) {
#pragma unroll
      for (int h = 0; h < 2; ++h) {
        *(f32x4*)(mrg + h * 260 + 4 + c * 8) = f32x4{acc[h][0], acc[h][1], acc[h][2], acc[h][3]};
        *(f32x4*)(mrg + h * 260 + 4 + c * 8 + 4) = f32x4{acc[h][4], acc[h][5], acc[h][6], acc[h][7]};
      }
      if ((lane & 7) == 0 && lane < 16) { mrg[(lane >> 3) * 260] = m; mrg[(lane >> 3) * 260 + 1] = l; }
    }
  }
  lds_barrier();
  {
    const int h = tid >> 8, d = tid & 255;
    const float* mrg = lds + L_MRG + h * 260;
    float M = -INFINITY;
#pragma unroll
    for (int ww = 0; ww < NW; ++ww) M = fmaxf(M, mrg[ww * 520]);
    float L = 0.f, o = 0.f;
#pragma unroll
    for (int ww = 0; ww < NW; ++ww) { const float wt = __builtin_amdgcn_exp2f(mrg[ww * 520] - M); L = fmaf(wt, mrg[ww * 520 + 1], L); o = fmaf(wt, mrg[ww * 520 + 4 + d], o); }
    GAS u64* slot = pg + (size_t)(pair * 4 + sp) * PG_STRIDE;
    st_gran(slot + h * 256 + d, T, __float_as_uint(o));
    if (d < 2) st_gran(slot + 512 + 2 * h + d, T, __float_as_uint(d == 0 ? M : L));
  }
  STAMP(5);
  {
    const int hs = sp >> 1, pj = lane >> 4, j = lane & 15;
    GAS u64* slot = pg + (size_t)(pair * 4 + pj) * PG_STRIDE;
    GAS u64* ga = slot + hs * 256 + (sp & 1) * 128 + 16 * w + j;
    GAS u64* gm = slot + 512 + 2 * hs + (j & 1);
    u64 a, ml;
    for (unsigned n = 0; n < SPIN_CAP; ++n) {
      a = ld_gran(ga); ml = ld_gran(gm);
      if (__all((unsigned)(a >> 32) == T && (unsigned)(ml >> 32) == T)) break;
      __builtin_amdgcn_s_sleep(1);
    }
    STAMP(6);
    const float mlv = __uint_as_float((unsigned)ml), oth = dpp<XOR1>(mlv);
    const float Mp = (j & 1) ? oth : mlv, Lp = (j & 1) ? mlv : oth;
    const float M = x32_max(x16_max(Mp));
    const float wt = __builtin_amdgcn_exp2f(Mp - M);
    const float L = x32_sum(x16_sum(wt * Lp));
    const float o = x32_sum(x16_sum(wt * __uint_as_float((unsigned)a)));
    const unsigned ob = f2bf(o / L);
    const unsigned pk = ob | (__builtin_bit_cast(unsigned, dpp<XOR1>(__builtin_bit_cast(float, ob))) << 16);
    float sa[8];
#pragma unroll
    for (int q = 0; q < 8; ++q) sa[q] = 0.f;
#pragma unroll
    for (int i = 0; i < 8; ++i) {
      const unsigned op = __builtin_amdgcn_readlane(pk, 2 * i);
#pragma unroll
      for (int q = 0; q < 4; ++q) { sa[2 * q] = dot2(wor[2 * i][q], op, sa[2 * q]); sa[2 * q + 1] = dot2(wor[2 * i + 1][q], op, sa[2 * q + 1]); }
    }
    if (lane < 56) { *(f32x4*)(lds + L_S + w * 448 + lane * 8) = f32x4{sa[0], sa[1], sa[2], sa[3]}; *(f32x4*)(lds + L_S + w * 448 + lane * 8 + 4) = f32x4{sa[4], sa[5], sa[6], sa[7]}; }
  }
  lds_barrier();
  if (tid < 448) {
    float s = 0.f, y = 0.f;
#pragma unroll
    for (int ww = 0; ww < 8; ++ww) s += lds[L_S + ww * 448 + tid];
#pragma unroll
    for (int ww = 0; ww < 7; ++ww) y += lds[L_Y + ww * 448 + tid];
    __builtin_amdgcn_raw_buffer_store_b128(v4u{__float_as_uint(s), T, __float_as_uint(y), T}, rs, WS_SG + (unsigned)(b * 32 + part) * (448 * 16) + tid * 16, 0, 16);
  }
  STAMP(7);
  if (part < 28) {
    const int pp = tid >> 4, col = tid & 15;
    const unsigned off = WS_SG + (unsigned)(b * 32 + pp) * (448 * 16) + (part * 16 + col) * 16;
    v4u g;
    for (unsigned n = 0; n < SPIN_CAP; ++n) {
      g = __builtin_amdgcn_raw_buffer_load_b128(rs, off, 0, 16);
      if (__all(g.y == T && g.w == T)) break;
      __builtin_amdgcn_s_sleep(1);
    }
    STAMP(8);
    const float s = x32_sum(x16_sum(__uint_as_float(g.x))), y = x32_sum(x16_sum(__uint_as_float(g.z)));
    if (lane < 16) *(f32x2*)(lds + L_FIN + (w * 16 + lane) * 2) = f32x2{s, y};
    lds_barrier();
    if (tid < 16) {
      float st = 0.f, yt = 0.f;
#pragma unroll
      for (int ww = 0; ww < 8; ++ww) { st += lds[L_FIN + (ww * 16 + tid) * 2]; yt += lds[L_FIN + (ww * 16 + tid) * 2 + 1]; }
      st = rbf(st);
      yt = rbf(rbf(yt) + b2v);
      const float g2 = rbf(yt / (1.f + __expf(-yt)));
      P.out[b * D + part * 16 + tid] = (unsigned short)f2bf(xin + rbf(st * g2));
    }
  }
  STAMP(9);
}

extern "C" void kernel_launch(void* const* d_in, const int* in_sizes, int n_in, void* d_out, int out_size, void* d_ws, size_t ws_size,
                              hipStream_t stream) {
  Params P;
  P.x = (const unsigned short*)d_in[0];
  P.kc = (const unsigned short*)d_in[1];
  P.vc = (const unsigned short*)d_in[2];
  P.wq = (const unsigned short*)d_in[3];
  P.wo = (const unsigned short*)d_in[4];
  P.w1 = (const unsigned short*)d_in[6];
  P.b1 = (const unsigned short*)d_in[7];
  P.w2 = (const unsigned short*)d_in[8];
  P.b2 = (const unsigned short*)d_in[9];
  P.out = (unsigned short*)d_out;
  P.ws = (unsigned char*)d_ws;
  static bool attr_set = false;
  if (!attr_set) { (void)hipFuncSetAttribute((const void*)fused_kernel, hipFuncAttributeMaxDynamicSharedMemorySize, LDS_BYTES); attr_set = true; }
  fused_kernel<<<NWG, NTHR, LDS_BYTES, stream>>>(P);
}
```

```cpp
#include <hip/hip_runtime.h>
#include <hip/hip_bf16.h>
#include <stdint.h>

typedef unsigned v4u __attribute__((ext_vector_type(4)));
typedef float f32x4 __attribute__((ext_vector_type(4)));
typedef float f32x2 __attribute__((ext_vector_type(2)));
typedef __attribute__((ext_vector_type(2))) __bf16 bf16x2;
typedef unsigned long long u64;
#define GAS __attribute__((address_space(1)))
#define RLX_AGENT __ATOMIC_RELAXED, __HIP_MEMORY_SCOPE_AGENT

constexpr int B = 8, D = 448, HKV = 8, SK = 8192, DH = 256, F = 1792;
constexpr int NWG = 256, NW = 8, NTHR = 512;
#define GS 28
constexpr int NDYN = (128 - 4 * GS) * 8;
constexpr unsigned SPIN_CAP = 1u << 22;
constexpr unsigned OOB = 0x80000000u;

constexpr unsigned WS_EPOCH = 0;
constexpr unsigned WS_CTR = 4096;
constexpr unsigned WS_QG = WS_CTR + 64 * 256;
constexpr unsigned WS_HG = WS_QG + 8 * 8 * 4 * 512 * 8;
constexpr unsigned WS_PG = WS_HG + 8 * 8 * 1792 * 8;
constexpr unsigned PG_STRIDE = 576;
constexpr unsigned WS_SG = WS_PG + 256 * PG_STRIDE * 8;
constexpr unsigned WS_END = WS_SG + 8 * 32 * 448 * 16;

__device__ __forceinline__ float bf2f(unsigned short b) { return __builtin_bit_cast(float, (unsigned)b << 16); }
__device__ __forceinline__ float rbf(float f) { return (float)(__bf16)f; }
__device__ __forceinline__ unsigned f2bf(float f) { return (unsigned)__builtin_bit_cast(unsigned short, (__bf16)f); }
__device__ __forceinline__ float lo16(unsigned u) { return __builtin_bit_cast(float, u << 16); }
__device__ __forceinline__ float hi16(unsigned u) { return __builtin_bit_cast(float, u & 0xffff0000u); }
__device__ __forceinline__ float dot2(unsigned k, unsigned q, float acc) {
  return __builtin_amdgcn_fdot2_f32_bf16(__builtin_bit_cast(bf16x2, k), __builtin_bit_cast(bf16x2, q), acc, false);
}
template <int CTRL> __device__ __forceinline__ float dpp(float x) {
  return __builtin_bit_cast(float, __builtin_amdgcn_mov_dpp(__builtin_bit_cast(int, x), CTRL, 0xf, 0xf, true));
}
constexpr int XOR1 = 0xB1, XOR2 = 0x4E, XOR7 = 0x141, XOR8 = 0x128;
__device__ __forceinline__ float x16_sum(float x) {
  auto s = __builtin_amdgcn_permlane16_swap(__float_as_uint(x), __float_as_uint(x), false, false);
  return __uint_as_float(s[0]) + __uint_as_float(s[1]);
}
__device__ __forceinline__ float x32_sum(float x) {
  auto s = __builtin_amdgcn_permlane32_swap(__float_as_uint(x), __float_as_uint(x), false, false);
  return __uint_as_float(s[0]) + __uint_as_float(s[1]);
}
__device__ __forceinline__ float x16_max(float x) {
  auto s = __builtin_amdgcn_permlane16_swap(__float_as_uint(x), __float_as_uint(x), false, false);
  return fmaxf(__uint_as_float(s[0]), __uint_as_float(s[1]));
}
__device__ __forceinline__ float x32_max(float x) {
  auto s = __builtin_amdgcn_permlane32_swap(__float_as_uint(x), __float_as_uint(x), false, false);
  return fmaxf(__uint_as_float(s[0]), __uint_as_float(s[1]));
}
__device__ __forceinline__ float tr32(float a, float b) {
  auto s = __builtin_amdgcn_permlane32_swap(__float_as_uint(a), __float_as_uint(b), false, false);
  return __uint_as_float(s[0]) + __uint_as_float(s[1]);
}
__device__ __forceinline__ float wave_sum(float x) {
  x += dpp<XOR1>(x); x += dpp<XOR2>(x); x += dpp<XOR7>(x); x += dpp<XOR8>(x); x = x16_sum(x); return x32_sum(x);
}
__device__ __forceinline__ v4u ldnt(const GAS v4u* p) { return __builtin_nontemporal_load(p); }
__device__ __forceinline__ void st_gran(GAS u64* g, unsigned tag, unsigned val) { __hip_atomic_store(g, ((u64)tag << 32) | val, RLX_AGENT); }
__device__ __forceinline__ u64 ld_gran(GAS u64* g) { return __hip_atomic_load(g, RLX_AGENT); }
__device__ __forceinline__ void lds_barrier() { asm volatile("s_waitcnt lgkmcnt(0)" ::: "memory"); __builtin_amdgcn_s_barrier(); asm volatile("" ::: "memory"); }
__device__ __forceinline__ __amdgpu_buffer_rsrc_t mk_rsrc(const void* p, unsigned bytes) { return __builtin_amdgcn_make_buffer_rsrc((void*)p, 0, bytes, 0x00020000); }
__device__ __forceinline__ void glds16(const void* gsrc, unsigned lds_dst) {
  unsigned keep;
  asm volatile("s_mov_b32 %0, m0\n\ts_mov_b32 m0, %2\n\ts_nop 0\n\tglobal_load_lds_dwordx4 %1, off\n\ts_mov_b32 m0, %0" : "=&s"(keep) : "v"(gsrc), "s"(lds_dst) : "memory");
}

#define STAMP(i) do { } while (0)
struct Params {
  const unsigned short *x, *kc, *vc, *wq, *wo, *w1, *b1, *w2, *b2;
  unsigned short* out;
  unsigned char* ws;
};

constexpr int L_WREG = 4096;
constexpr int L_XNP = 8 * L_WREG;
constexpr int L_Q = L_XNP + 2016;
constexpr int L_Y = L_Q + 256;
constexpr int L_END = L_Y + 7 * 448;
constexpr int LDS_BYTES = L_END * 4;
static_assert(LDS_BYTES <= 160 * 1024 && L_Y % 4 == 0 && L_Q % 4 == 0, "");

__global__ void __launch_bounds__(NTHR, 2) fused_kernel(Params P) {
  extern __shared__ __attribute__((aligned(16))) float lds[];
  unsigned* lds_u = (unsigned*)lds;
  const int tid = threadIdx.x, lane = tid & 63, w = __builtin_amdgcn_readfirstlane(tid >> 6);
  const int bid = blockIdx.x;
  const int pair = bid >> 2, sp = bid & 3, b = pair >> 3, kvh = pair & 7;
  const int part = kvh * 4 + sp;
  const int c = lane & 31;
  GAS unsigned char* ws = (GAS unsigned char*)P.ws;
  GAS u64* qg = (GAS u64*)(ws + WS_QG);
  GAS u64* hg = (GAS u64*)(ws + WS_HG);
  GAS u64* pg = (GAS u64*)(ws + WS_PG);
  const __amdgpu_buffer_rsrc_t rs = mk_rsrc(P.ws, WS_END);
  float* wreg = lds + w * L_WREG;
  unsigned* wreg_u = lds_u + w * L_WREG;
  const bool has_f1 = (bid & 7) != 7;
  const int f1u = (bid >> 3) * 7 + (bid & 7), f1cb = f1u >> 3, f1kq = f1u & 7;

  STAMP(0);
  const unsigned epoch = __hip_atomic_load((GAS unsigned*)(ws + WS_EPOCH), RLX_AGENT);
  const unsigned l56 = lane < 56 ? lane * 16 : OOB;
  const v4u xv = __builtin_amdgcn_raw_buffer_load_b128(mk_rsrc(P.x, B * D * 2), l56 + w * (D * 2), 0, 0);
  unsigned wl[4], wh[4];
  {
    const __amdgpu_buffer_rsrc_t r = mk_rsrc(P.wq, D * 4096 * 2);
    const unsigned o0 = tid < 448 ? (unsigned)((sp * 112 + 2 * (w * 2 + (lane >> 5))) * 4096 + kvh * 512 + b * 64) * 2 + c * 4 : OOB;
#pragma unroll
    for (int j = 0; j < 4; ++j) {
      wl[j] = __builtin_amdgcn_raw_buffer_load_b32(r, o0 + (unsigned)(28 * j) * 8192, 0, 0);
      wh[j] = __builtin_amdgcn_raw_buffer_load_b32(r, o0 + (unsigned)(28 * j + 1) * 8192, 0, 0);
    }
  }
  {
    const v4u z = {0u, 0u, 0u, 0u};
    if (tid < (int)PG_STRIDE / 2) __builtin_amdgcn_raw_buffer_store_b128(z, rs, WS_PG + (unsigned)bid * (PG_STRIDE * 8) + tid * 16, 0, 16);
    if (tid < 448) __builtin_amdgcn_raw_buffer_store_b128(z, rs, WS_SG + (unsigned)(b * 32 + part) * (448 * 16) + tid * 16, 0, 16);
    if (has_f1 && tid < 256) __builtin_amdgcn_raw_buffer_store_b128(z, rs, WS_HG + (unsigned)((((tid >> 5) * 8 + f1kq) * 1792 + f1cb * 64) * 8) + (tid & 31) * 16, 0, 16);
    if (sp == 0 && tid == 511) __builtin_amdgcn_raw_buffer_store_b32(0u, rs, WS_CTR + pair * 256, 0, 16);
  }
  __builtin_amdgcn_sched_barrier(0);
  __builtin_amdgcn_s_barrier();
  const size_t base = (size_t)pair * SK * DH;
  constexpr int USTR = NW * 64, TSTR = NW * 256;
  const GAS v4u* kbase = (const GAS v4u*)(P.kc + base) + lane;
  const GAS v4u* vbase = (const GAS v4u*)(P.vc + base) + lane;
  const GAS v4u* kp = kbase + (size_t)sp * GS * TSTR + w * 64;
  const GAS v4u* vp = vbase + (size_t)sp * GS * TSTR + w * 64;
  v4u KA[4], VA[4], KB[4], VB[4], KC[4], VC[4], Kb[4], Vb[4];
#define LD4(dst, ptr) { _Pragma("unroll") for (int u = 0; u < 4; ++u) dst[u] = ldnt(ptr + u * USTR); ptr += TSTR; __builtin_amdgcn_sched_barrier(0); }
  LD4(KA, kp)
  {
    float v[8];
#pragma unroll
    for (int j = 0; j < 4; ++j) { v[2 * j] = lo16(xv[j]); v[2 * j + 1] = hi16(xv[j]); }
    float s = 0.f;
#pragma unroll
    for (int j = 0; j < 8; ++j) s += v[j];
    const float mean = wave_sum(s) * (1.f / D);
    float q = 0.f;
#pragma unroll
    for (int j = 0; j < 8; ++j) { const float d = v[j] - mean; q = fmaf(d, d, q); }
    if (lane >= 56) q = 0.f;
    const float var = rbf(wave_sum(q) * (1.f / D));
    const float mb = rbf(mean);
    const float inv = 1.f / rbf(sqrtf(rbf(var + 1e-5f)));
    if (lane < 56) {
#pragma unroll
      for (int j = 0; j < 4; ++j) {
        const unsigned lo = f2bf(rbf(v[2 * j] - mb) * inv), hi = f2bf(rbf(v[2 * j + 1] - mb) * inv);
        lds_u[L_XNP + (lane * 4 + j) * 8 + lane * 4 + w] = lo | (hi << 16);
      }
    }
  }
  STAMP(14);
  lds_barrier();
  STAMP(1);
  if (w < 7) {
    float x[16];
#pragma unroll
    for (int v = 0; v < 16; ++v) x[v] = 0.f;
#pragma unroll
    for (int j = 0; j < 4; ++j) {
      const int kq = sp * 56 + w * 2 + (lane >> 5) + 14 * j;
      const unsigned* xr = lds_u + L_XNP + kq * 8 + (kq >> 2) * 4;
      const v4u xa = *(const v4u*)xr, xb = *(const v4u*)(xr + 4);
      const unsigned pa = __builtin_amdgcn_perm(wh[j], wl[j], 0x05040100u), pb = __builtin_amdgcn_perm(wh[j], wl[j], 0x07060302u);
#pragma unroll
      for (int bb = 0; bb < 4; ++bb) { x[bb] = dot2(pa, xa[bb], x[bb]); x[4 + bb] = dot2(pa, xb[bb], x[4 + bb]); x[8 + bb] = dot2(pb, xa[bb], x[8 + bb]); x[12 + bb] = dot2(pb, xb[bb], x[12 + bb]); }
    }
#pragma unroll
    for (int u = 0; u < 8; ++u) wreg[u * 64 + 2 * c + (lane >> 5)] = tr32(x[u], x[u + 8]);
  }
  STAMP(11);
  asm volatile("s_waitcnt vmcnt(4)" ::: "memory");
  LD4(VA, vp)
  lds_barrier();
  STAMP(12);
  const unsigned T = (epoch + 1u) | 0x80000000u;
  {
    float v0 = 0.f;
#pragma unroll
    for (int wv = 0; wv < 7; ++wv) v0 += lds[wv * L_WREG + w * 64 + lane];
    st_gran(qg + ((kvh * 8 + w) * 4 + sp) * 512 + b * 64 + lane, T, __float_as_uint(v0));
  }
  STAMP(2);
  LD4(KB, kp)
  {
    GAS u64* g = qg + (size_t)(kvh * 8 + b) * 2048 + tid;
    u64 g0 = ld_gran(g), g1 = ld_gran(g + 512), g2 = ld_gran(g + 1024), g3 = ld_gran(g + 1536);
    LD4(VB, vp)
    for (unsigned n = 0; n < SPIN_CAP; ++n) {
      if (__all((unsigned)(g0 >> 32) == T && (unsigned)(g1 >> 32) == T && (unsigned)(g2 >> 32) == T && (unsigned)(g3 >> 32) == T)) break;
      g0 = ld_gran(g); g1 = ld_gran(g + 512); g2 = ld_gran(g + 1024); g3 = ld_gran(g + 1536);
    }
    const float qs = ((__uint_as_float((unsigned)g0) + __uint_as_float((unsigned)g1)) + __uint_as_float((unsigned)g2)) + __uint_as_float((unsigned)g3);
    ((unsigned short*)(lds_u + L_Q))[tid] = (unsigned short)f2bf(qs);
  }
  LD4(KC, kp)
  lds_barrier();
  STAMP(3);
  v4u qv[2];
  qv[0] = *(const v4u*)(lds_u + L_Q + c * 4);
  qv[1] = *(const v4u*)(lds_u + L_Q + 128 + c * 4);

  float acc[2][8];
#pragma unroll
  for (int h = 0; h < 2; ++h)
#pragma unroll
    for (int d = 0; d < 8; ++d) acc[h][d] = 0.f;
  float m = -INFINITY, l = 0.f, p;
  constexpr float C2 = 0.0625f * 1.4426950408889634f;
#define TR_STEP(HALF, CTRL, BIT) _Pragma("unroll") for (int u = 0; u < (HALF); ++u) { \
        const float keep = (lane & (BIT)) ? x[u + (HALF)] : x[u]; const float send = (lane & (BIT)) ? x[u] : x[u + (HALF)]; \
        x[u] = keep + dpp<CTRL>(send); }
#define QKSM(KX) { float x[8]; \
      _Pragma("unroll") for (int h = 0; h < 2; ++h) \
        _Pragma("unroll") for (int i = 0; i < 4; ++i) { float d = 0.f; \
          _Pragma("unroll") for (int k = 0; k < 4; ++k) d = dot2(KX[i][k], qv[h][k], d); \
          x[h * 4 + i] = d; } \
      TR_STEP(4, XOR8, 8) TR_STEP(2, XOR7, 4) TR_STEP(1, XOR2, 2) \
      float s = x[0]; s += dpp<XOR1>(s); s = x16_sum(s); \
      s = rbf(s) * C2; \
      if (__any(s > m)) { \
        float pm = s; pm = fmaxf(pm, dpp<XOR2>(pm)); pm = fmaxf(pm, dpp<XOR7>(pm)); pm = x32_max(pm); \
        const float mn = fmaxf(m, pm); \
        const float alpha = __builtin_amdgcn_exp2f(m - mn); \
        m = mn; l *= alpha; \
        const float a0 = dpp<0x150>(alpha), a1 = dpp<0x158>(alpha); \
        _Pragma("unroll") for (int d = 0; d < 8; ++d) { acc[0][d] *= a0; acc[1][d] *= a1; } \
      } \
      p = __builtin_amdgcn_exp2f(s - m); \
      l += p; \
      __builtin_amdgcn_sched_barrier(0); }
#define PV1(VX, I) { float vf[8]; _Pragma("unroll") for (int k = 0; k < 4; ++k) { vf[2 * k] = lo16(VX[I][k]); vf[2 * k + 1] = hi16(VX[I][k]); } \
        const float p0 = dpp<0x150 + 2 * I>(p), p1 = dpp<0x158 + 2 * I>(p); \
        _Pragma("unroll") for (int d = 0; d < 8; ++d) { acc[0][d] = fmaf(p0, vf[d], acc[0][d]); acc[1][d] = fmaf(p1, vf[d], acc[1][d]); } }
#define PV(VX) { PV1(VX, 0) PV1(VX, 1) PV1(VX, 2) PV1(VX, 3) __builtin_amdgcn_sched_barrier(0); }
#define SEG(N) _Pragma("unroll 1") for (int t = 0; t < (N); ++t) { LD4(Kb, kp) QKSM(Kb) LD4(Vb, vp) PV(Vb) }
#define SEGD(N) _Pragma("unroll 1") for (int t = 0; t < (N); ++t) { \
      glds16(dsrc, dlds); glds16(dsrc + dstr, dlds + 1024); dsrc += 2 * dstr; dlds += 2048; \
      LD4(Kb, kp) QKSM(Kb) LD4(Vb, vp) PV(Vb) }
  LD4(VC, vp)
  QKSM(KA) PV(VA) QKSM(KB) PV(VB)
  LD4(Kb, kp)
  QKSM(KC) PV(VC)
  QKSM(Kb) LD4(Vb, vp) PV(Vb)
  STAMP(16);
  const int l55 = lane < 56 ? lane : 55;
  {
    const char* dsrc; size_t dstr; unsigned dlds = (unsigned)w * (L_WREG * 4);
    if (w < 7) { dsrc = (const char*)(P.w2 + (size_t)(56 * part + 8 * w) * D + l55 * 8); dstr = D * 2; }
    else { dsrc = (const char*)(P.w1 + (size_t)(56 * f1kq + (lane >> 3)) * F + (has_f1 ? f1cb : 0) * 64 + (lane & 7) * 8); dstr = has_f1 ? (size_t)8 * F * 2 : 0; }
    SEGD(3)
    if (w == 7) dstr = 0;
    SEGD(1)
  }
  STAMP(17);
  if (w == 7 && has_f1) {
    float x[16];
#pragma unroll
    for (int v = 0; v < 16; ++v) x[v] = 0.f;
#pragma unroll 2
    for (int j = 0; j < 14; ++j) {
      const int kl = (lane >> 5) + 2 * j, kq = f1kq * 28 + kl;
      const unsigned* xr = lds_u + L_XNP + kq * 8 + (kq >> 2) * 4;
      const v4u xa = *(const v4u*)xr, xb = *(const v4u*)(xr + 4);
      const unsigned a0 = wreg_u[(2 * kl) * 32 + c], a1 = wreg_u[(2 * kl + 1) * 32 + c];
      const unsigned pa = __builtin_amdgcn_perm(a1, a0, 0x05040100u), pb = __builtin_amdgcn_perm(a1, a0, 0x07060302u);
#pragma unroll
      for (int bb = 0; bb < 4; ++bb) { x[bb] = dot2(pa, xa[bb], x[bb]); x[4 + bb] = dot2(pa, xb[bb], x[4 + bb]); x[8 + bb] = dot2(pb, xa[bb], x[8 + bb]); x[12 + bb] = dot2(pb, xb[bb], x[12 + bb]); }
    }
    GAS u64* hd = hg + (size_t)f1kq * 1792 + f1cb * 64 + 2 * c + (lane >> 5);
#pragma unroll
    for (int u = 0; u < 8; ++u) st_gran(hd + (size_t)u * (8 * 1792), T, __float_as_uint(tr32(x[u], x[u + 8])));
  }
  SEG(3)
  STAMP(18);
  {
    GAS u64* rgp[4]; u64 rg[4];
    const int hcol = 56 * part + 8 * (w < 7 ? w : 0) + (lane & 7);
    {
      const int pu = (hcol >> 6) * 8 + (lane >> 3), pbid = (pu / 7) * 8 + pu % 7;
#pragma unroll
      for (int j = 0; j < 4; ++j) {
        const int i = w < 7 ? pbid : lane * 4 + j;
        rgp[j] = (w < 7 && j == 0) ? hg + (size_t)(b * 8 + (lane >> 3)) * 1792 + hcol : qg + (size_t)((((i >> 2) & 7) * 8) * 4 + (i & 3)) * 512 + (i >> 5) * 64;
        rg[j] = ld_gran(rgp[j]);
      }
    }
    const float b1v = bf2f(P.b1[hcol]);
    SEG(1)
    for (unsigned n = 0; n < SPIN_CAP; ++n) {
      if (__all((unsigned)(rg[0] >> 32) == T && (unsigned)(rg[1] >> 32) == T && (unsigned)(rg[2] >> 32) == T && (unsigned)(rg[3] >> 32) == T)) break;
      __builtin_amdgcn_s_sleep(1);
#pragma unroll
      for (int j = 0; j < 4; ++j) rg[j] = ld_gran(rgp[j]);
    }
    if (w < 7) {
      float hv = __uint_as_float((unsigned)rg[0]);
      hv += dpp<XOR8>(hv); hv = x16_sum(hv); hv = x32_sum(hv);
      const float t = rbf(rbf(hv) + b1v);
      const float u = 0.7978845608028654f * (t + 0.044715f * t * t * t);
      const float th = 1.f - 2.f / (__expf(2.f * u) + 1.f);
      const unsigned hb = f2bf(0.5f * t * (1.f + th));
      const unsigned hpk = hb | (__builtin_bit_cast(unsigned, dpp<XOR1>(__builtin_bit_cast(float, hb))) << 16);
      float ya[8];
#pragma unroll
      for (int j = 0; j < 8; ++j) ya[j] = 0.f;
#pragma unroll
      for (int i = 0; i < 4; ++i) {
        const unsigned hp = __builtin_amdgcn_readlane(hpk, 2 * i);
        const v4u r0 = *(const v4u*)(wreg_u + (2 * i) * 256 + lane * 4), r1 = *(const v4u*)(wreg_u + (2 * i + 1) * 256 + lane * 4);
#pragma unroll
        for (int q = 0; q < 4; ++q) {
          ya[2 * q] = dot2(__builtin_amdgcn_perm(r1[q], r0[q], 0x05040100u), hp, ya[2 * q]);
          ya[2 * q + 1] = dot2(__builtin_amdgcn_perm(r1[q], r0[q], 0x07060302u), hp, ya[2 * q + 1]);
        }
      }
      if (lane < 56) { *(f32x4*)(lds + L_Y + w * 448 + lane * 8) = f32x4{ya[0], ya[1], ya[2], ya[3]}; *(f32x4*)(lds + L_Y + w * 448 + lane * 8 + 4) = f32x4{ya[4], ya[5], ya[6], ya[7]}; }
    } else {
      if (bid == 0 && lane == 0) __hip_atomic_store((GAS unsigned*)(ws + WS_EPOCH), epoch + 1u, RLX_AGENT);
    }
    asm volatile("s_waitcnt lgkmcnt(0)" ::: "memory");
  }
  STAMP(19);
  {
    const char* dsrc = (const char*)(P.wo + (size_t)((kvh * 2 + (sp >> 1)) * 256 + (sp & 1) * 128 + 16 * w) * D + l55 * 8);
    const size_t dstr = D * 2; unsigned dlds = (unsigned)w * (L_WREG * 4);
    SEGD(8)
  }
  STAMP(20);
  v4u wor[16];
#pragma unroll
  for (int i = 0; i < 8; ++i) {
    const v4u r0 = *(const v4u*)(wreg_u + (2 * i) * 256 + lane * 4), r1 = *(const v4u*)(wreg_u + (2 * i + 1) * 256 + lane * 4);
#pragma unroll
    for (int q = 0; q < 4; ++q) {
      wor[2 * i][q] = __builtin_amdgcn_perm(r1[q], r0[q], 0x05040100u);
      wor[2 * i + 1][q] = __builtin_amdgcn_perm(r1[q], r0[q], 0x07060302u);
    }
  }
#pragma unroll
  for (int r = 0; r < 16; ++r) asm volatile("" : "+v"(wor[r]));
  float xin = 0.f, b2v = 0.f;
  if (part < 28 && tid < 16) { xin = bf2f(P.x[b * D + part * 16 + tid]); b2v = bf2f(P.b2[part * 16 + tid]); }
  STAMP(21);
  SEG(GS - 21)
  STAMP(22);
  {
    const v4u rc = {(unsigned)(size_t)P.ws, (unsigned)((size_t)P.ws >> 32) & 0xffffu, WS_END, 0x00020000u};
    const unsigned coff = lane == 0 ? WS_CTR + pair * 256 : OOB;
    unsigned tick = 1;
    asm volatile("buffer_atomic_add %0, %1, %2, 0 offen sc0" : "+v"(tick) : "v"(coff), "s"(rc) : "memory");
    SEG(1)
    asm volatile("s_waitcnt vmcnt(0)" : "+v"(tick));
    unsigned cur = __builtin_amdgcn_readfirstlane(tick);
#pragma unroll 1
    while (cur < (unsigned)NDYN) {
      tick = 1;
      asm volatile("buffer_atomic_add %0, %1, %2, 0 offen sc0" : "+v"(tick) : "v"(coff), "s"(rc) : "memory");
      const size_t toff = (size_t)(4 * GS + (cur >> 3)) * TSTR + (cur & 7) * 64;
      kp = kbase + toff; vp = vbase + toff;
      LD4(Kb, kp) QKSM(Kb) LD4(Vb, vp) PV(Vb)
      asm volatile("s_waitcnt vmcnt(0)" : "+v"(tick));
      cur = __builtin_amdgcn_readfirstlane(tick);
    }
  }
  STAMP(4);
#pragma unroll
  for (int h = 0; h < 2; ++h)
#pragma unroll
    for (int d = 0; d < 8; ++d) acc[h][d] = x32_sum(acc[h][d]);
  l += dpp<XOR2>(l); l += dpp<XOR7>(l); l = x32_sum(l);
  if (lane < 32) {
#pragma unroll
    for (int h = 0; h < 2; ++h) {
      *(f32x4*)(wreg + h * 260 + 4 + c * 8) = f32x4{acc[h][0], acc[h][1], acc[h][2], acc[h][3]};
      *(f32x4*)(wreg + h * 260 + 4 + c * 8 + 4) = f32x4{acc[h][4], acc[h][5], acc[h][6], acc[h][7]};
    }
    if ((lane & 7) == 0 && lane < 16) { wreg[(lane >> 3) * 260] = m; wreg[(lane >> 3) * 260 + 1] = l; }
  }
  lds_barrier();
  {
    const int h = tid >> 8, d = tid & 255;
    const float* mrg = lds + h * 260;
    float M = -INFINITY;
#pragma unroll
    for (int ww = 0; ww < NW; ++ww) M = fmaxf(M, mrg[ww * L_WREG]);
    float L = 0.f, o = 0.f;
#pragma unroll
    for (int ww = 0; ww < NW; ++ww) { const float wt = __builtin_amdgcn_exp2f(mrg[ww * L_WREG] - M); L = fmaf(wt, mrg[ww * L_WREG + 1], L); o = fmaf(wt, mrg[ww * L_WREG + 4 + d], o); }
    GAS u64* slot = pg + (size_t)bid * PG_STRIDE;
    st_gran(slot + h * 256 + d, T, __float_as_uint(o));
    if (d < 2) st_gran(slot + 512 + 2 * h + d, T, __float_as_uint(d == 0 ? M : L));
  }
  STAMP(5);
  {
    const int hs = sp >> 1, pj = lane >> 4, j = lane & 15;
    GAS u64* slot = pg + (size_t)(pair * 4 + pj) * PG_STRIDE;
    GAS u64* ga = slot + hs * 256 + (sp & 1) * 128 + 16 * w + j;
    GAS u64* gm = slot + 512 + 2 * hs + (j & 1);
    u64 a, ml;
#pragma unroll 1
    for (unsigned n = 0; n < SPIN_CAP; ++n) {
      a = ld_gran(ga); ml = ld_gran(gm);
      if (__all((unsigned)(a >> 32) == T && (unsigned)(ml >> 32) == T)) break;
    }
    STAMP(6);
    const float mlv = __uint_as_float((unsigned)ml), oth = dpp<XOR1>(mlv);
    const float Mp = (j & 1) ? oth : mlv, Lp = (j & 1) ? mlv : oth;
    const float M = x32_max(x16_max(Mp));
    const float wt = __builtin_amdgcn_exp2f(Mp - M);
    const float L = x32_sum(x16_sum(wt * Lp));
    const float o = x32_sum(x16_sum(wt * __uint_as_float((unsigned)a)));
    const unsigned ob = f2bf(o / L);
    const unsigned pk = ob | (__builtin_bit_cast(unsigned, dpp<XOR1>(__builtin_bit_cast(float, ob))) << 16);
    float sa[8];
#pragma unroll
    for (int q = 0; q < 8; ++q) sa[q] = 0.f;
#pragma unroll
    for (int i = 0; i < 8; ++i) {
      const unsigned op = __builtin_amdgcn_readlane(pk, 2 * i);
#pragma unroll
      for (int q = 0; q < 4; ++q) { sa[2 * q] = dot2(wor[2 * i][q], op, sa[2 * q]); sa[2 * q + 1] = dot2(wor[2 * i + 1][q], op, sa[2 * q + 1]); }
    }
    if (lane < 56) { *(f32x4*)(wreg + 1024 + lane * 8) = f32x4{sa[0], sa[1], sa[2], sa[3]}; *(f32x4*)(wreg + 1024 + lane * 8 + 4) = f32x4{sa[4], sa[5], sa[6], sa[7]}; }
  }
  lds_barrier();
  if (tid < 448) {
    float s = 0.f, y = 0.f;
#pragma unroll
    for (int ww = 0; ww < 8; ++ww) s += lds[ww * L_WREG + 1024 + tid];
#pragma unroll
    for (int ww = 0; ww < 7; ++ww) y += lds[L_Y + ww * 448 + tid];
    __builtin_amdgcn_raw_buffer_store_b128(v4u{__float_as_uint(s), T, __float_as_uint(y), T}, rs, WS_SG + (unsigned)(b * 32 + part) * (448 * 16) + tid * 16, 0, 16);
  }
  STAMP(7);
  if (part < 28) {
    const int pp = tid >> 4, col = tid & 15;
    const unsigned off = WS_SG + (unsigned)(b * 32 + pp) * (448 * 16) + (part * 16 + col) * 16;
    v4u g;
#pragma unroll 1
    for (unsigned n = 0; n < SPIN_CAP; ++n) {
      asm volatile("" ::: "memory");
      g = __builtin_amdgcn_raw_buffer_load_b128(rs, off, 0, 16);
      if (__all(g.y == T && g.w == T)) break;
    }
    STAMP(8);
    const float s = x32_sum(x16_sum(__uint_as_float(g.x))), y = x32_sum(x16_sum(__uint_as_float(g.z)));
    if (lane < 16) *(f32x2*)(wreg + 2048 + lane * 2) = f32x2{s, y};
    lds_barrier();
    if (tid < 16) {
      float st = 0.f, yt = 0.f;
#pragma unroll
      for (int ww = 0; ww < 8; ++ww) { st += lds[ww * L_WREG + 2048 + tid * 2]; yt += lds[ww * L_WREG + 2048 + tid * 2 + 1]; }
      st = rbf(st);
      yt = rbf(rbf(yt) + b2v);
      const float g2 = rbf(yt / (1.f + __expf(-yt)));
      P.out[b * D + part * 16 + tid] = (unsigned short)f2bf(xin + rbf(st * g2));
    }
  }
  STAMP(9);
}

extern "C" void kernel_launch(void* const* d_in, const int* in_sizes, int n_in, void* d_out, int out_size, void* d_ws, size_t ws_size,
                              hipStream_t stream) {
  Params P;
  P.x = (const unsigned short*)d_in[0];
  P.kc = (const unsigned short*)d_in[1];
  P.vc = (const unsigned short*)d_in[2];
  P.wq = (const unsigned short*)d_in[3];
  P.wo = (const unsigned short*)d_in[4];
  P.w1 = (const unsigned short*)d_in[6];
  P.b1 = (const unsigned short*)d_in[7];
  P.w2 = (const unsigned short*)d_in[8];
  P.b2 = (const unsigned short*)d_in[9];
  P.out = (unsigned short*)d_out;
  P.ws = (unsigned char*)d_ws;
  (void)hipFuncSetAttribute((const void*)fused_kernel, hipFuncAttributeMaxDynamicSharedMemorySize, LDS_BYTES);
  fused_kernel<<<NWG, NTHR, LDS_BYTES, stream>>>(P);
}
```

```cpp
#include <hip/hip_runtime.h>
#include <hip/hip_bf16.h>
#include <stdint.h>

typedef unsigned v4u __attribute__((ext_vector_type(4)));
typedef float f32x4 __attribute__((ext_vector_type(4)));
typedef float f32x2 __attribute__((ext_vector_type(2)));
typedef __attribute__((ext_vector_type(2))) __bf16 bf16x2;
typedef unsigned long long u64;
#define GAS __attribute__((address_space(1)))
#define RLX_AGENT __ATOMIC_RELAXED, __HIP_MEMORY_SCOPE_AGENT

constexpr int B = 8, D = 448, HKV = 8, SK = 8192, DH = 256, F = 1792;
constexpr int NWG = 256, NW = 8, NTHR = 512;
#define GS 28
constexpr int NDYN = (128 - 4 * GS) * 8;
constexpr unsigned SPIN_CAP = 1u << 22;
constexpr unsigned OOB = 0x80000000u;

constexpr unsigned WS_EPOCH = 0;
constexpr unsigned WS_CTR = 4096;
constexpr unsigned WS_QG = WS_CTR + 64 * 256;
constexpr unsigned WS_HG = WS_QG + 8 * 8 * 4 * 512 * 8;
constexpr unsigned WS_PG = WS_HG + 8 * 8 * 1792 * 8;
constexpr unsigned PG_STRIDE = 576;
constexpr unsigned WS_SG = WS_PG + 256 * PG_STRIDE * 8;
constexpr unsigned WS_END = WS_SG + 8 * 32 * 448 * 16;

__device__ __forceinline__ float bf2f(unsigned short b) { return __builtin_bit_cast(float, (unsigned)b << 16); }
__device__ __forceinline__ float rbf(float f) { return (float)(__bf16)f; }
__device__ __forceinline__ unsigned f2bf(float f) { return (unsigned)__builtin_bit_cast(unsigned short, (__bf16)f); }
__device__ __forceinline__ float lo16(unsigned u) { return __builtin_bit_cast(float, u << 16); }
__device__ __forceinline__ float hi16(unsigned u) { return __builtin_bit_cast(float, u & 0xffff0000u); }
__device__ __forceinline__ float dot2(unsigned k, unsigned q, float acc) {
  return __builtin_amdgcn_fdot2_f32_bf16(__builtin_bit_cast(bf16x2, k), __builtin_bit_cast(bf16x2, q), acc, false);
}
template <int CTRL> __device__ __forceinline__ float dpp(float x) {
  return __builtin_bit_cast(float, __builtin_amdgcn_mov_dpp(__builtin_bit_cast(int, x), CTRL, 0xf, 0xf, true));
}
constexpr int XOR1 = 0xB1, XOR2 = 0x4E, XOR7 = 0x141, XOR8 = 0x128;
__device__ __forceinline__ float x16_sum(float x) {
  auto s = __builtin_amdgcn_permlane16_swap(__float_as_uint(x), __float_as_uint(x), false, false);
  return __uint_as_float(s[0]) + __uint_as_float(s[1]);
}
__device__ __forceinline__ float x32_sum(float x) {
  auto s = __builtin_amdgcn_permlane32_swap(__float_as_uint(x), __float_as_uint(x), false, false);
  return __uint_as_float(s[0]) + __uint_as_float(s[1]);
}
__device__ __forceinline__ float x16_max(float x) {
  auto s = __builtin_amdgcn_permlane16_swap(__float_as_uint(x), __float_as_uint(x), false, false);
  return fmaxf(__uint_as_float(s[0]), __uint_as_float(s[1]));
}
__device__ __forceinline__ float x32_max(float x) {
  auto s = __builtin_amdgcn_permlane32_swap(__float_as_uint(x), __float_as_uint(x), false, false);
  return fmaxf(__uint_as_float(s[0]), __uint_as_float(s[1]));
}
__device__ __forceinline__ float tr32(float a, float b) {
  auto s = __builtin_amdgcn_permlane32_swap(__float_as_uint(a), __float_as_uint(b), false, false);
  return __uint_as_float(s[0]) + __uint_as_float(s[1]);
}
__device__ __forceinline__ float wave_sum(float x) {
  x += dpp<XOR1>(x); x += dpp<XOR2>(x); x += dpp<XOR7>(x); x += dpp<XOR8>(x); x = x16_sum(x); return x32_sum(x);
}
__device__ __forceinline__ v4u ldnt(const GAS v4u* p) { return __builtin_nontemporal_load(p); }
__device__ __forceinline__ void st_gran(GAS u64* g, unsigned tag, unsigned val) { __hip_atomic_store(g, ((u64)tag << 32) | val, RLX_AGENT); }
__device__ __forceinline__ u64 ld_gran(GAS u64* g) { return __hip_atomic_load(g, RLX_AGENT); }
__device__ __forceinline__ void lds_barrier() { asm volatile("s_waitcnt lgkmcnt(0)" ::: "memory"); __builtin_amdgcn_s_barrier(); asm volatile("" ::: "memory"); }
__device__ __forceinline__ __amdgpu_buffer_rsrc_t mk_rsrc(const void* p, unsigned bytes) { return __builtin_amdgcn_make_buffer_rsrc((void*)p, 0, bytes, 0x00020000); }
__device__ __forceinline__ void glds16(const void* gsrc, unsigned lds_dst) {
  unsigned keep;
  asm volatile("s_mov_b32 %0, m0\n\ts_mov_b32 m0, %2\n\ts_nop 0\n\tglobal_load_lds_dwordx4 %1, off nt\n\ts_mov_b32 m0, %0" : "=&s"(keep) : "v"(gsrc), "s"(lds_dst) : "memory");
}

#define TST() do { } while (0)
#define STAMP(i) do { } while (0)
struct Params {
  const unsigned short *x, *kc, *vc, *wq, *wo, *w1, *b1, *w2, *b2;
  unsigned short* out;
  unsigned char* ws;
};

constexpr int L_WREG = 2048;
constexpr int L_XNP = 8 * L_WREG;
constexpr int L_Q = L_XNP + 2016;
constexpr int L_Y = L_Q + 256;
constexpr int L_SC = L_Y + 7 * 448;
constexpr int NSLOT = 8 * GS + NDYN;
constexpr int L_TID = L_SC + NSLOT * 16;
constexpr int L_MX = L_TID + (NDYN > 0 ? NDYN : 4);
constexpr int L_CTR = L_MX + 16;
constexpr int L_END = L_CTR + 4;
constexpr int LDS_BYTES = L_END * 4;
static_assert(LDS_BYTES <= 160 * 1024 && LDS_BYTES > 80 * 1024 && L_Y % 4 == 0 && L_Q % 4 == 0 && L_SC % 4 == 0, "");

__global__ void __launch_bounds__(NTHR, 2) fused_kernel(Params P) {
  extern __shared__ __attribute__((aligned(16))) float lds[];
  unsigned* lds_u = (unsigned*)lds;
  const int tid = threadIdx.x, lane = tid & 63, w = __builtin_amdgcn_readfirstlane(tid >> 6);
  const int bid = blockIdx.x;
  const int pair = bid >> 2, sp = bid & 3, b = pair >> 3, kvh = pair & 7;
  const int part = kvh * 4 + sp;
  const int c = lane & 31;
  GAS unsigned char* ws = (GAS unsigned char*)P.ws;
  GAS u64* qg = (GAS u64*)(ws + WS_QG);
  GAS u64* hg = (GAS u64*)(ws + WS_HG);
  GAS u64* pg = (GAS u64*)(ws + WS_PG);
  const __amdgpu_buffer_rsrc_t rs = mk_rsrc(P.ws, WS_END);
  float* wreg = lds + w * L_WREG;
  unsigned* wreg_u = lds_u + w * L_WREG;
  const bool has_f1 = (bid & 7) != 7;
  const int f1u = (bid >> 3) * 7 + (bid & 7), f1cb = f1u >> 3, f1kq = f1u & 7;

  STAMP(0);
  const unsigned epoch = __hip_atomic_load((GAS unsigned*)(ws + WS_EPOCH), RLX_AGENT);
  const unsigned l56 = lane < 56 ? lane * 16 : OOB;
  const v4u xv = __builtin_amdgcn_raw_buffer_load_b128(mk_rsrc(P.x, B * D * 2), l56 + w * (D * 2), 0, 2);
  unsigned wl[4], wh[4];
  {
    const __amdgpu_buffer_rsrc_t r = mk_rsrc(P.wq, D * 4096 * 2);
    const unsigned o0 = tid < 448 ? (unsigned)((sp * 112 + 2 * (w * 2 + (lane >> 5))) * 4096 + kvh * 512 + b * 64) * 2 + c * 4 : OOB;
#pragma unroll
    for (int j = 0; j < 4; ++j) {
      wl[j] = __builtin_amdgcn_raw_buffer_load_b32(r, o0 + (unsigned)(28 * j) * 8192, 0, 2);
      wh[j] = __builtin_amdgcn_raw_buffer_load_b32(r, o0 + (unsigned)(28 * j + 1) * 8192, 0, 2);
    }
  }
  {
    const v4u z = {0u, 0u, 0u, 0u};
    if (tid < (int)PG_STRIDE / 2) __builtin_amdgcn_raw_buffer_store_b128(z, rs, WS_PG + (unsigned)bid * (PG_STRIDE * 8) + tid * 16, 0, 16);
    if (tid < 448) __builtin_amdgcn_raw_buffer_store_b128(z, rs, WS_SG + (unsigned)(b * 32 + part) * (448 * 16) + tid * 16, 0, 16);
    if (has_f1 && tid < 256) __builtin_amdgcn_raw_buffer_store_b128(z, rs, WS_HG + (unsigned)((((tid >> 5) * 8 + f1kq) * 1792 + f1cb * 64) * 8) + (tid & 31) * 16, 0, 16);
    if (sp == 0 && tid == 511) __builtin_amdgcn_raw_buffer_store_b32(0u, rs, WS_CTR + pair * 256, 0, 16);
    if (tid == 0) { lds_u[L_CTR] = 0u; lds_u[L_CTR + 1] = 16u; }
  }
  __builtin_amdgcn_sched_barrier(0);
  __builtin_amdgcn_s_barrier();
  const size_t base = (size_t)pair * SK * DH;
  constexpr int USTR = NW * 64, TSTR = NW * 256;
  const GAS v4u* kbase = (const GAS v4u*)(P.kc + base) + lane;
  const GAS v4u* vbase = (const GAS v4u*)(P.vc + base) + lane;
  const GAS v4u* kp = kbase + (size_t)sp * GS * TSTR + w * 64;
  v4u K0[4], K1[4], K2[4], K3[4], K4[4], K5[4], Kb[4];
#define LD4(dst, ptr) { _Pragma("unroll") for (int u = 0; u < 4; ++u) dst[u] = ldnt(ptr + u * USTR); ptr += TSTR; __builtin_amdgcn_sched_barrier(0); }
  LD4(K0, kp)
  {
    float v[8];
#pragma unroll
    for (int j = 0; j < 4; ++j) { v[2 * j] = lo16(xv[j]); v[2 * j + 1] = hi16(xv[j]); }
    float s = 0.f;
#pragma unroll
    for (int j = 0; j < 8; ++j) s += v[j];
    const float mean = wave_sum(s) * (1.f / D);
    float q = 0.f;
#pragma unroll
    for (int j = 0; j < 8; ++j) { const float d = v[j] - mean; q = fmaf(d, d, q); }
    if (lane >= 56) q = 0.f;
    const float var = rbf(wave_sum(q) * (1.f / D));
    const float mb = rbf(mean);
    const float inv = 1.f / rbf(sqrtf(rbf(var + 1e-5f)));
    if (lane < 56) {
#pragma unroll
      for (int j = 0; j < 4; ++j) {
        const unsigned lo = f2bf(rbf(v[2 * j] - mb) * inv), hi = f2bf(rbf(v[2 * j + 1] - mb) * inv);
        lds_u[L_XNP + (lane * 4 + j) * 8 + lane * 4 + w] = lo | (hi << 16);
      }
    }
  }
  STAMP(14);
  lds_barrier();
  STAMP(1);
  if (w < 7) {
    float x[16];
#pragma unroll
    for (int v = 0; v < 16; ++v) x[v] = 0.f;
#pragma unroll
    for (int j = 0; j < 4; ++j) {
      const int kq = sp * 56 + w * 2 + (lane >> 5) + 14 * j;
      const unsigned* xr = lds_u + L_XNP + kq * 8 + (kq >> 2) * 4;
      const v4u xa = *(const v4u*)xr, xb = *(const v4u*)(xr + 4);
      const unsigned pa = __builtin_amdgcn_perm(wh[j], wl[j], 0x05040100u), pb = __builtin_amdgcn_perm(wh[j], wl[j], 0x07060302u);
#pragma unroll
      for (int bb = 0; bb < 4; ++bb) { x[bb] = dot2(pa, xa[bb], x[bb]); x[4 + bb] = dot2(pa, xb[bb], x[4 + bb]); x[8 + bb] = dot2(pb, xa[bb], x[8 + bb]); x[12 + bb] = dot2(pb, xb[bb], x[12 + bb]); }
    }
#pragma unroll
    for (int u = 0; u < 8; ++u) wreg[u * 64 + 2 * c + (lane >> 5)] = tr32(x[u], x[u + 8]);
  }
  STAMP(11);
  asm volatile("s_waitcnt vmcnt(4)" ::: "memory");
  LD4(K1, kp)
  lds_barrier();
  STAMP(12);
  const unsigned T = (epoch + 1u) | 0x80000000u;
  {
    float v0 = 0.f;
#pragma unroll
    for (int wv = 0; wv < 7; ++wv) v0 += lds[wv * L_WREG + w * 64 + lane];
    st_gran(qg + ((kvh * 8 + w) * 4 + sp) * 512 + b * 64 + lane, T, __float_as_uint(v0));
  }
  STAMP(2);
  {
    GAS u64* g = qg + (size_t)(kvh * 8 + b) * 2048 + tid;
#define QPOLL() { g0 = ld_gran(g); g1 = ld_gran(g + 512); g2 = ld_gran(g + 1024); g3 = ld_gran(g + 1536); }
#define QOK() __all((unsigned)(g0 >> 32) == T && (unsigned)(g1 >> 32) == T && (unsigned)(g2 >> 32) == T && (unsigned)(g3 >> 32) == T)
    u64 g0, g1, g2, g3;
    QPOLL()
    LD4(K2, kp)
    if (!QOK()) QPOLL()
    LD4(K3, kp)
    for (unsigned n = 0; n < SPIN_CAP; ++n) {
      if (QOK()) break;
      QPOLL()
    }
    const float qs = ((__uint_as_float((unsigned)g0) + __uint_as_float((unsigned)g1)) + __uint_as_float((unsigned)g2)) + __uint_as_float((unsigned)g3);
    ((unsigned short*)(lds_u + L_Q))[tid] = (unsigned short)f2bf(qs);
  }
  LD4(K4, kp)
  lds_barrier();
  STAMP(3);
  LD4(K5, kp)
  v4u qv[2];
  qv[0] = *(const v4u*)(lds_u + L_Q + c * 4);
  qv[1] = *(const v4u*)(lds_u + L_Q + 128 + c * 4);

  constexpr float C2 = 0.0625f * 1.4426950408889634f;
  float mx = -INFINITY;
  int slot = w;
  const int sidx = (lane >> 5) * 8 + ((lane >> 1) & 7);
  const bool swr = (lane & 0x11) == 0;
#define TR_STEP(HALF, CTRL, BIT) _Pragma("unroll") for (int u = 0; u < (HALF); ++u) { \
        const float keep = (lane & (BIT)) ? x[u + (HALF)] : x[u]; const float send = (lane & (BIT)) ? x[u] : x[u + (HALF)]; \
        x[u] = keep + dpp<CTRL>(send); }
#define QK(KX) { float x[8]; \
      _Pragma("unroll") for (int h = 0; h < 2; ++h) \
        _Pragma("unroll") for (int i = 0; i < 4; ++i) { float d = 0.f; \
          _Pragma("unroll") for (int k = 0; k < 4; ++k) d = dot2(KX[i][k], qv[h][k], d); \
          x[h * 4 + i] = d; } \
      TR_STEP(4, XOR8, 8) TR_STEP(2, XOR7, 4) TR_STEP(1, XOR2, 2) \
      float s = x[0]; s += dpp<XOR1>(s); s = x16_sum(s); \
      s = rbf(s) * C2; \
      mx = fmaxf(mx, s); \
      if (swr) lds[L_SC + slot * 16 + sidx] = s; \
      slot += 8; \
      __builtin_amdgcn_sched_barrier(0); TST(); }
#define SEGA(N) _Pragma("unroll 1") for (int t = 0; t < (N); ++t) { LD4(Kb, kp) QK(Kb) }
#define DMA2() { glds16(dsrc, dlds); glds16(dsrc + dstr, dlds + 1024); dsrc += 2 * dstr; dlds += 2048; }
#define SEGAD(N) _Pragma("unroll 1") for (int t = 0; t < (N); ++t) { DMA2() LD4(Kb, kp) QK(Kb) }
  QK(K0) QK(K1) QK(K2)
  LD4(Kb, kp)
  QK(K3) QK(K4) QK(K5)
  QK(Kb)
  STAMP(16);
  const int l55 = lane < 56 ? lane : 55;
  {
    const char* dsrc; size_t dstr; unsigned dlds = (unsigned)w * (L_WREG * 4);
    if (w < 7) { dsrc = (const char*)(P.w2 + (size_t)(56 * part + 8 * w) * D + l55 * 8); dstr = D * 2; }
    else { dsrc = (const char*)(P.w1 + (size_t)(56 * f1kq + (lane >> 3)) * F + (has_f1 ? f1cb : 0) * 64 + (lane & 7) * 8); dstr = has_f1 ? (size_t)8 * F * 2 : 0; }
    SEGAD(3)
    if (w == 7) dstr = 0;
    SEGAD(1)
  }
  STAMP(17);
  if (w == 7 && has_f1) {
    float x[16];
#pragma unroll
    for (int v = 0; v < 16; ++v) x[v] = 0.f;
#pragma unroll 2
    for (int j = 0; j < 14; ++j) {
      const int kl = (lane >> 5) + 2 * j, kq = f1kq * 28 + kl;
      const unsigned* xr = lds_u + L_XNP + kq * 8 + (kq >> 2) * 4;
      const v4u xa = *(const v4u*)xr, xb = *(const v4u*)(xr + 4);
      const unsigned a0 = wreg_u[(2 * kl) * 32 + c], a1 = wreg_u[(2 * kl + 1) * 32 + c];
      const unsigned pa = __builtin_amdgcn_perm(a1, a0, 0x05040100u), pb = __builtin_amdgcn_perm(a1, a0, 0x07060302u);
#pragma unroll
      for (int bb = 0; bb < 4; ++bb) { x[bb] = dot2(pa, xa[bb], x[bb]); x[4 + bb] = dot2(pa, xb[bb], x[4 + bb]); x[8 + bb] = dot2(pb, xa[bb], x[8 + bb]); x[12 + bb] = dot2(pb, xb[bb], x[12 + bb]); }
    }
    GAS u64* hd = hg + (size_t)f1kq * 1792 + f1cb * 64 + 2 * c + (lane >> 5);
#pragma unroll
    for (int u = 0; u < 8; ++u) st_gran(hd + (size_t)u * (8 * 1792), T, __float_as_uint(tr32(x[u], x[u + 8])));
  }
  SEGA(7)
  STAMP(18);
  {
    GAS u64* rgp[4]; u64 rg[4];
    const int hcol = 56 * part + 8 * (w < 7 ? w : 0) + (lane & 7);
    {
      const int pu = (hcol >> 6) * 8 + (lane >> 3), pbid = (pu / 7) * 8 + pu % 7;
#pragma unroll
      for (int j = 0; j < 4; ++j) {
        const int i = w < 7 ? pbid : lane * 4 + j;
        rgp[j] = (w < 7 && j == 0) ? hg + (size_t)(b * 8 + (lane >> 3)) * 1792 + hcol : qg + (size_t)((((i >> 2) & 7) * 8) * 4 + (i & 3)) * 512 + (i >> 5) * 64;
        rg[j] = ld_gran(rgp[j]);
      }
    }
    const float b1v = bf2f(P.b1[hcol]);
    SEGA(1)
    for (unsigned n = 0; n < SPIN_CAP; ++n) {
      if (__all((unsigned)(rg[0] >> 32) == T && (unsigned)(rg[1] >> 32) == T && (unsigned)(rg[2] >> 32) == T && (unsigned)(rg[3] >> 32) == T)) break;
      __builtin_amdgcn_s_sleep(1);
#pragma unroll
      for (int j = 0; j < 4; ++j) rg[j] = ld_gran(rgp[j]);
    }
    if (w < 7) {
      float hv = __uint_as_float((unsigned)rg[0]);
      hv += dpp<XOR8>(hv); hv = x16_sum(hv); hv = x32_sum(hv);
      const float t = rbf(rbf(hv) + b1v);
      const float u = 0.7978845608028654f * (t + 0.044715f * t * t * t);
      const float th = 1.f - 2.f / (__expf(2.f * u) + 1.f);
      const unsigned hb = f2bf(0.5f * t * (1.f + th));
      const unsigned hpk = hb | (__builtin_bit_cast(unsigned, dpp<XOR1>(__builtin_bit_cast(float, hb))) << 16);
      float ya[8];
#pragma unroll
      for (int j = 0; j < 8; ++j) ya[j] = 0.f;
#pragma unroll
      for (int i = 0; i < 4; ++i) {
        const unsigned hp = __builtin_amdgcn_readlane(hpk, 2 * i);
        const v4u r0 = *(const v4u*)(wreg_u + (2 * i) * 256 + lane * 4), r1 = *(const v4u*)(wreg_u + (2 * i + 1) * 256 + lane * 4);
#pragma unroll
        for (int q = 0; q < 4; ++q) {
          ya[2 * q] = dot2(__builtin_amdgcn_perm(r1[q], r0[q], 0x05040100u), hp, ya[2 * q]);
          ya[2 * q + 1] = dot2(__builtin_amdgcn_perm(r1[q], r0[q], 0x07060302u), hp, ya[2 * q + 1]);
        }
      }
      if (lane < 56) { *(f32x4*)(lds + L_Y + w * 448 + lane * 8) = f32x4{ya[0], ya[1], ya[2], ya[3]}; *(f32x4*)(lds + L_Y + w * 448 + lane * 8 + 4) = f32x4{ya[4], ya[5], ya[6], ya[7]}; }
    } else {
      if (bid == 0 && lane == 0) __hip_atomic_store((GAS unsigned*)(ws + WS_EPOCH), epoch + 1u, RLX_AGENT);
    }
    asm volatile("s_waitcnt lgkmcnt(0)" ::: "memory");
  }
  STAMP(19);
  SEGA(GS - 20)
  {
    const v4u rc = {(unsigned)(size_t)P.ws, (unsigned)((size_t)P.ws >> 32) & 0xffffu, WS_END, 0x00020000u};
    const unsigned coff = lane == 0 ? WS_CTR + pair * 256 : OOB;
    unsigned tick = 1;
    asm volatile("buffer_atomic_add %0, %1, %2, 0 offen sc0" : "+v"(tick) : "v"(coff), "s"(rc) : "memory");
    SEGA(1)
    asm volatile("s_waitcnt vmcnt(0)" : "+v"(tick));
    unsigned cur = __builtin_amdgcn_readfirstlane(tick);
#pragma unroll 1
    while (cur < (unsigned)NDYN) {
      tick = 1;
      asm volatile("buffer_atomic_add %0, %1, %2, 0 offen sc0" : "+v"(tick) : "v"(coff), "s"(rc) : "memory");
      kp = kbase + (size_t)(4 * GS + (cur >> 3)) * TSTR + (cur & 7) * 64;
      unsigned n = 0;
      if (lane == 0) { n = __hip_atomic_fetch_add(lds_u + L_CTR, 1u, __ATOMIC_RELAXED, __HIP_MEMORY_SCOPE_WORKGROUP); lds_u[L_TID + n] = cur; }
      slot = 8 * GS + __builtin_amdgcn_readfirstlane(n);
      LD4(Kb, kp) QK(Kb)
      asm volatile("s_waitcnt vmcnt(0)" : "+v"(tick));
      cur = __builtin_amdgcn_readfirstlane(tick);
    }
  }
  STAMP(20);
  mx = fmaxf(mx, dpp<XOR2>(mx)); mx = fmaxf(mx, dpp<XOR7>(mx)); mx = x32_max(mx);
  if (lane == 0 || lane == 8) lds[L_MX + w * 2 + (lane >> 3)] = mx;
  v4u VA[4], VB[4], Vb[4];
  const GAS v4u* vp = vbase + (size_t)sp * GS * TSTR + w * 64;
  LD4(VA, vp) LD4(VB, vp)
  lds_barrier();
  STAMP(21);
  float M;
  {
    const int hh = (lane >> 3) & 1;
    M = lds[L_MX + hh];
#pragma unroll
    for (int ww = 1; ww < 8; ++ww) M = fmaxf(M, lds[L_MX + ww * 2 + hh]);
  }
  const int nslots = 8 * GS + (int)lds_u[L_CTR];
  float acc[2][8];
#pragma unroll
  for (int h = 0; h < 2; ++h)
#pragma unroll
    for (int d = 0; d < 8; ++d) acc[h][d] = 0.f;
  float l = 0.f;
#define PV1(VX, I) { float vf[8]; _Pragma("unroll") for (int k = 0; k < 4; ++k) { vf[2 * k] = lo16(VX[I][k]); vf[2 * k + 1] = hi16(VX[I][k]); } \
        const float p0 = dpp<0x150 + 2 * I>(p), p1 = dpp<0x158 + 2 * I>(p); \
        _Pragma("unroll") for (int d = 0; d < 8; ++d) { acc[0][d] = fmaf(p0, vf[d], acc[0][d]); acc[1][d] = fmaf(p1, vf[d], acc[1][d]); } }
#define PV(VX, SL) { const float p = __builtin_amdgcn_exp2f(lds[L_SC + (SL) * 16 + sidx] - M); l += p; \
        PV1(VX, 0) PV1(VX, 1) PV1(VX, 2) PV1(VX, 3) __builtin_amdgcn_sched_barrier(0); TST(); }
#define BTICKET() ({ unsigned n_ = 0; if (lane == 0) n_ = __hip_atomic_fetch_add(lds_u + L_CTR + 1, 1u, __ATOMIC_RELAXED, __HIP_MEMORY_SCOPE_WORKGROUP); (int)__builtin_amdgcn_readfirstlane(n_); })
#define BTILE() { if (cur < nslots) { \
        const int tl = cur < 8 * GS ? sp * GS * 8 + cur : 4 * GS * 8 + (int)lds_u[L_TID + (cur < 8 * GS ? 0 : cur - 8 * GS)]; \
        vp = vbase + (size_t)(tl >> 3) * TSTR + (tl & 7) * 64; \
        const int sl = cur; cur = BTICKET(); \
        LD4(Vb, vp) PV(Vb, sl) } }
  int cur = BTICKET();
  PV(VA, w) PV(VB, 8 + w)
  v4u wor[16];
  {
    const char* dsrc = (const char*)(P.wo + (size_t)((kvh * 2 + (sp >> 1)) * 256 + (sp & 1) * 128 + 16 * w) * D + l55 * 8);
    const size_t dstr = D * 2;
#pragma unroll
    for (int hf = 0; hf < 2; ++hf) {
      unsigned dlds = (unsigned)w * (L_WREG * 4);
#pragma unroll 1
      for (int t = 0; t < 4; ++t) { DMA2() BTILE() }
      asm volatile("s_waitcnt vmcnt(0)" ::: "memory");
#pragma unroll
      for (int i = 0; i < 4; ++i) {
        const v4u r0 = *(const v4u*)(wreg_u + (2 * i) * 256 + lane * 4), r1 = *(const v4u*)(wreg_u + (2 * i + 1) * 256 + lane * 4);
#pragma unroll
        for (int q = 0; q < 4; ++q) {
          wor[8 * hf + 2 * i][q] = __builtin_amdgcn_perm(r1[q], r0[q], 0x05040100u);
          wor[8 * hf + 2 * i + 1][q] = __builtin_amdgcn_perm(r1[q], r0[q], 0x07060302u);
        }
      }
#pragma unroll
      for (int r = 0; r < 8; ++r) asm volatile("" : "+v"(wor[8 * hf + r]));
      asm volatile("s_waitcnt lgkmcnt(0)" ::: "memory");
    }
  }
  float xin = 0.f, b2v = 0.f;
  if (part < 28 && tid < 16) { xin = bf2f(P.x[b * D + part * 16 + tid]); b2v = bf2f(P.b2[part * 16 + tid]); }
  STAMP(22);
#pragma unroll 1
  while (cur < nslots) BTILE()
  STAMP(4);
#pragma unroll
  for (int h = 0; h < 2; ++h)
#pragma unroll
    for (int d = 0; d < 8; ++d) acc[h][d] = x32_sum(acc[h][d]);
  l += dpp<XOR2>(l); l += dpp<XOR7>(l); l = x32_sum(l);
  if (lane < 32) {
#pragma unroll
    for (int h = 0; h < 2; ++h) {
      *(f32x4*)(wreg + h * 260 + 4 + c * 8) = f32x4{acc[h][0], acc[h][1], acc[h][2], acc[h][3]};
      *(f32x4*)(wreg + h * 260 + 4 + c * 8 + 4) = f32x4{acc[h][4], acc[h][5], acc[h][6], acc[h][7]};
    }
    if ((lane & 7) == 0 && lane < 16) wreg[(lane >> 3) * 260] = l;
  }
  if (lane == 0 || lane == 8) lds[L_MX + w * 2 + (lane >> 3)] = M;
  lds_barrier();
  {
    const int h = tid >> 8, d = tid & 255;
    const float* mrg = lds + h * 260;
    float L = 0.f, o = 0.f;
#pragma unroll
    for (int ww = 0; ww < NW; ++ww) { L += mrg[ww * L_WREG]; o += mrg[ww * L_WREG + 4 + d]; }
    GAS u64* slotp = pg + (size_t)bid * PG_STRIDE;
    st_gran(slotp + h * 256 + d, T, __float_as_uint(o));
    if (d < 2) st_gran(slotp + 512 + 2 * h + d, T, __float_as_uint(d == 0 ? lds[L_MX + h] : L));
  }
  STAMP(5);
  {
    const int hs = sp >> 1, pj = lane >> 4, j = lane & 15;
    GAS u64* slot = pg + (size_t)(pair * 4 + pj) * PG_STRIDE;
    GAS u64* ga = slot + hs * 256 + (sp & 1) * 128 + 16 * w + j;
    GAS u64* gm = slot + 512 + 2 * hs + (j & 1);
    u64 a, ml;
#pragma unroll 1
    for (unsigned n = 0; n < SPIN_CAP; ++n) {
      a = ld_gran(ga); ml = ld_gran(gm);
      if (__all((unsigned)(a >> 32) == T && (unsigned)(ml >> 32) == T)) break;
    }
    STAMP(6);
    const float mlv = __uint_as_float((unsigned)ml), oth = dpp<XOR1>(mlv);
    const float Mp = (j & 1) ? oth : mlv, Lp = (j & 1) ? mlv : oth;
    const float M = x32_max(x16_max(Mp));
    const float wt = __builtin_amdgcn_exp2f(Mp - M);
    const float L = x32_sum(x16_sum(wt * Lp));
    const float o = x32_sum(x16_sum(wt * __uint_as_float((unsigned)a)));
    const unsigned ob = f2bf(o / L);
    const unsigned pk = ob | (__builtin_bit_cast(unsigned, dpp<XOR1>(__builtin_bit_cast(float, ob))) << 16);
    float sa[8];
#pragma unroll
    for (int q = 0; q < 8; ++q) sa[q] = 0.f;
#pragma unroll
    for (int i = 0; i < 8; ++i) {
      const unsigned op = __builtin_amdgcn_readlane(pk, 2 * i);
#pragma unroll
      for (int q = 0; q < 4; ++q) { sa[2 * q] = dot2(wor[2 * i][q], op, sa[2 * q]); sa[2 * q + 1] = dot2(wor[2 * i + 1][q], op, sa[2 * q + 1]); }
    }
    if (lane < 56) { *(f32x4*)(wreg + 1024 + lane * 8) = f32x4{sa[0], sa[1], sa[2], sa[3]}; *(f32x4*)(wreg + 1024 + lane * 8 + 4) = f32x4{sa[4], sa[5], sa[6], sa[7]}; }
  }
  lds_barrier();
  if (tid < 448) {
    float s = 0.f, y = 0.f;
#pragma unroll
    for (int ww = 0; ww < 8; ++ww) s += lds[ww * L_WREG + 1024 + tid];
#pragma unroll
    for (int ww = 0; ww < 7; ++ww) y += lds[L_Y + ww * 448 + tid];
    __builtin_amdgcn_raw_buffer_store_b128(v4u{__float_as_uint(s), T, __float_as_uint(y), T}, rs, WS_SG + (unsigned)(b * 32 + part) * (448 * 16) + tid * 16, 0, 16);
  }
  STAMP(7);
  if (part < 28) {
    const int pp = tid >> 4, col = tid & 15;
    const unsigned off = WS_SG + (unsigned)(b * 32 + pp) * (448 * 16) + (part * 16 + col) * 16;
    v4u g;
#pragma unroll 1
    for (unsigned n = 0; n < SPIN_CAP; ++n) {
      asm volatile("" ::: "memory");
      g = __builtin_amdgcn_raw_buffer_load_b128(rs, off, 0, 16);
      if (__all(g.y == T && g.w == T)) break;
    }
    STAMP(8);
    const float s = x32_sum(x16_sum(__uint_as_float(g.x))), y = x32_sum(x16_sum(__uint_as_float(g.z)));
    if (lane < 16) *(f32x2*)(wreg + 1536 + lane * 2) = f32x2{s, y};
    lds_barrier();
    if (tid < 16) {
      float st = 0.f, yt = 0.f;
#pragma unroll
      for (int ww = 0; ww < 8; ++ww) { st += lds[ww * L_WREG + 1536 + tid * 2]; yt += lds[ww * L_WREG + 1536 + tid * 2 + 1]; }
      st = rbf(st);
      yt = rbf(rbf(yt) + b2v);
      const float g2 = rbf(yt / (1.f + __expf(-yt)));
      P.out[b * D + part * 16 + tid] = (unsigned short)f2bf(xin + rbf(st * g2));
    }
  }
  STAMP(9);
}

extern "C" void kernel_launch(void* const* d_in, const int* in_sizes, int n_in, void* d_out, int out_size, void* d_ws, size_t ws_size,
                              hipStream_t stream) {
  Params P;
  P.x = (const unsigned short*)d_in[0];
  P.kc = (const unsigned short*)d_in[1];
  P.vc = (const unsigned short*)d_in[2];
  P.wq = (const unsigned short*)d_in[3];
  P.wo = (const unsigned short*)d_in[4];
  P.w1 = (const unsigned short*)d_in[6];
  P.b1 = (const unsigned short*)d_in[7];
  P.w2 = (const unsigned short*)d_in[8];
  P.b2 = (const unsigned short*)d_in[9];
  P.out = (unsigned short*)d_out;
  P.ws = (unsigned char*)d_ws;
  (void)hipFuncSetAttribute((const void*)fused_kernel, hipFuncAttributeMaxDynamicSharedMemorySize, LDS_BYTES);
  fused_kernel<<<NWG, NTHR, LDS_BYTES, stream>>>(P);
}
```

```cpp
#include <hip/hip_runtime.h>
#include <hip/hip_bf16.h>
#include <stdint.h>

typedef unsigned v4u __attribute__((ext_vector_type(4)));
typedef float f32x4 __attribute__((ext_vector_type(4)));
typedef float f32x2 __attribute__((ext_vector_type(2)));
typedef __attribute__((ext_vector_type(2))) __bf16 bf16x2;
typedef unsigned long long u64;
#define GAS __attribute__((address_space(1)))
#define RLX_AGENT __ATOMIC_RELAXED, __HIP_MEMORY_SCOPE_AGENT

constexpr int B = 8, D = 448, HKV = 8, SK = 8192, DH = 256, F = 1792;
constexpr int NWG = 256, NW = 8, NTHR = 512;
constexpr int GS = 32;
constexpr unsigned SPIN_CAP = 1u << 17;
constexpr unsigned OOB = 0x80000000u;

constexpr unsigned WS_EPOCH = 0;
constexpr unsigned WS_QG = 4096;
constexpr unsigned WS_HG = WS_QG + 8 * 8 * 4 * 512 * 8;
constexpr unsigned WS_PG = WS_HG + 8 * 8 * 1792 * 8;
constexpr unsigned PG_STRIDE = 576;
constexpr unsigned WS_SG = WS_PG + 256 * PG_STRIDE * 8;
constexpr unsigned WS_END = WS_SG + 8 * 32 * 448 * 16;

__device__ __forceinline__ float bf2f(unsigned short b) { return __builtin_bit_cast(float, (unsigned)b << 16); }
__device__ __forceinline__ float rbf(float f) { return (float)(__bf16)f; }
__device__ __forceinline__ unsigned f2bf(float f) { return (unsigned)__builtin_bit_cast(unsigned short, (__bf16)f); }
__device__ __forceinline__ float lo16(unsigned u) { return __builtin_bit_cast(float, u << 16); }
__device__ __forceinline__ float hi16(unsigned u) { return __builtin_bit_cast(float, u & 0xffff0000u); }
__device__ __forceinline__ float dot2(unsigned k, unsigned q, float acc) {
  return __builtin_amdgcn_fdot2_f32_bf16(__builtin_bit_cast(bf16x2, k), __builtin_bit_cast(bf16x2, q), acc, false);
}
template <int CTRL> __device__ __forceinline__ float dpp(float x) {
  return __builtin_bit_cast(float, __builtin_amdgcn_mov_dpp(__builtin_bit_cast(int, x), CTRL, 0xf, 0xf, true));
}
constexpr int XOR1 = 0xB1, XOR2 = 0x4E, XOR7 = 0x141, XOR8 = 0x128;
__device__ __forceinline__ float x16_sum(float x) {
  auto s = __builtin_amdgcn_permlane16_swap(__float_as_uint(x), __float_as_uint(x), false, false);
  return __uint_as_float(s[0]) + __uint_as_float(s[1]);
}
__device__ __forceinline__ float x32_sum(float x) {
  auto s = __builtin_amdgcn_permlane32_swap(__float_as_uint(x), __float_as_uint(x), false, false);
  return __uint_as_float(s[0]) + __uint_as_float(s[1]);
}
__device__ __forceinline__ float x16_max(float x) {
  auto s = __builtin_amdgcn_permlane16_swap(__float_as_uint(x), __float_as_uint(x), false, false);
  return fmaxf(__uint_as_float(s[0]), __uint_as_float(s[1]));
}
__device__ __forceinline__ float x32_max(float x) {
  auto s = __builtin_amdgcn_permlane32_swap(__float_as_uint(x), __float_as_uint(x), false, false);
  return fmaxf(__uint_as_float(s[0]), __uint_as_float(s[1]));
}
__device__ __forceinline__ float tr32(float a, float b) {
  auto s = __builtin_amdgcn_permlane32_swap(__float_as_uint(a), __float_as_uint(b), false, false);
  return __uint_as_float(s[0]) + __uint_as_float(s[1]);
}
__device__ __forceinline__ float wave_sum(float x) {
  x += dpp<XOR1>(x); x += dpp<XOR2>(x); x += dpp<XOR7>(x); x += dpp<XOR8>(x); x = x16_sum(x); return x32_sum(x);
}
__device__ __forceinline__ v4u ldnt(const GAS v4u* p) { return __builtin_nontemporal_load(p); }
__device__ __forceinline__ void st_gran(GAS u64* g, unsigned tag, unsigned val) { __hip_atomic_store(g, ((u64)tag << 32) | val, RLX_AGENT); }
__device__ __forceinline__ u64 ld_gran(GAS u64* g) { return __hip_atomic_load(g, RLX_AGENT); }
__device__ __forceinline__ void lds_barrier() { asm volatile("s_waitcnt lgkmcnt(0)" ::: "memory"); __builtin_amdgcn_s_barrier(); asm volatile("" ::: "memory"); }
__device__ __forceinline__ __amdgpu_buffer_rsrc_t mk_rsrc(const void* p, unsigned bytes) { return __builtin_amdgcn_make_buffer_rsrc((void*)p, 0, bytes, 0x00020000); }
__device__ __forceinline__ void glds16(const void* gsrc, unsigned lds_dst) {
  unsigned keep;
  asm volatile("s_mov_b32 %0, m0\n\ts_mov_b32 m0, %2\n\ts_nop 0\n\tglobal_load_lds_dwordx4 %1, off nt\n\ts_mov_b32 m0, %0" : "=&s"(keep) : "v"(gsrc), "s"(lds_dst) : "memory");
}

#define TST() do { } while (0)
#define STAMP(i) do { } while (0)
struct Params {
  const unsigned short *x, *kc, *vc, *wq, *wo, *w1, *b1, *w2, *b2;
  unsigned short* out;
  unsigned char* ws;
};

constexpr int L_WREG = 2048;
constexpr int L_XNP = 8 * L_WREG;
constexpr int L_Q = L_XNP + 2016;
constexpr int L_Y = L_Q + 256;
constexpr int L_SC = L_Y + 7 * 448;
constexpr int NSLOT = 8 * GS;
constexpr int L_CTR = L_SC + NSLOT * 16;
constexpr int L_END = L_CTR + 4;
constexpr int LDS_BYTES = L_END * 4;
static_assert(LDS_BYTES <= 160 * 1024 && LDS_BYTES > 80 * 1024 && L_Y % 4 == 0 && L_Q % 4 == 0 && L_SC % 4 == 0, "");

__global__ void __launch_bounds__(NTHR, 2) fused_kernel(Params P) {
  extern __shared__ __attribute__((aligned(16))) float lds[];
  unsigned* lds_u = (unsigned*)lds;
  const int tid = threadIdx.x, lane = tid & 63, w = __builtin_amdgcn_readfirstlane(tid >> 6);
  const int bid = blockIdx.x;
  const int pair = bid >> 2, sp = bid & 3, b = pair >> 3, kvh = pair & 7;
  const int part = kvh * 4 + sp;
  const int c = lane & 31;
  GAS unsigned char* ws = (GAS unsigned char*)P.ws;
  GAS u64* qg = (GAS u64*)(ws + WS_QG);
  GAS u64* hg = (GAS u64*)(ws + WS_HG);
  GAS u64* pg = (GAS u64*)(ws + WS_PG);
  const __amdgpu_buffer_rsrc_t rs = mk_rsrc(P.ws, WS_END);
  float* wreg = lds + w * L_WREG;
  unsigned* wreg_u = lds_u + w * L_WREG;
  const bool has_f1 = (bid & 7) != 7;
  const int f1u = (bid >> 3) * 7 + (bid & 7), f1cb = f1u >> 3, f1kq = f1u & 7;

  STAMP(0);
  const unsigned epoch = __hip_atomic_load((GAS unsigned*)(ws + WS_EPOCH), RLX_AGENT);
  const unsigned l56 = lane < 56 ? lane * 16 : OOB;
  const v4u xv = __builtin_amdgcn_raw_buffer_load_b128(mk_rsrc(P.x, B * D * 2), l56 + w * (D * 2), 0, 2);
  unsigned wl[4], wh[4];
  {
    const __amdgpu_buffer_rsrc_t r = mk_rsrc(P.wq, D * 4096 * 2);
    const unsigned o0 = tid < 448 ? (unsigned)((sp * 112 + 2 * (w * 2 + (lane >> 5))) * 4096 + kvh * 512 + b * 64) * 2 + c * 4 : OOB;
#pragma unroll
    for (int j = 0; j < 4; ++j) {
      wl[j] = __builtin_amdgcn_raw_buffer_load_b32(r, o0 + (unsigned)(28 * j) * 8192, 0, 2);
      wh[j] = __builtin_amdgcn_raw_buffer_load_b32(r, o0 + (unsigned)(28 * j + 1) * 8192, 0, 2);
    }
  }
  {
    const v4u z = {0u, 0u, 0u, 0u};
    if (tid < (int)PG_STRIDE / 2) __builtin_amdgcn_raw_buffer_store_b128(z, rs, WS_PG + (unsigned)bid * (PG_STRIDE * 8) + tid * 16, 0, 16);
    if (tid < 448) __builtin_amdgcn_raw_buffer_store_b128(z, rs, WS_SG + (unsigned)(b * 32 + part) * (448 * 16) + tid * 16, 0, 16);
    if (has_f1 && tid < 256) __builtin_amdgcn_raw_buffer_store_b128(z, rs, WS_HG + (unsigned)((((tid >> 5) * 8 + f1kq) * 1792 + f1cb * 64) * 8) + (tid & 31) * 16, 0, 16);
    if (tid == 0) lds_u[L_CTR] = 0u;
  }
  __builtin_amdgcn_sched_barrier(0);
  __builtin_amdgcn_s_barrier();
  const size_t base = (size_t)pair * SK * DH;
  constexpr int USTR = NW * 64, TSTR = NW * 256;
  const GAS v4u* kbase = (const GAS v4u*)(P.kc + base) + lane;
  const GAS v4u* vbase = (const GAS v4u*)(P.vc + base) + lane;
  const GAS v4u* kp = kbase + (size_t)sp * GS * TSTR + w * 64;
  v4u K0[4], K1[4], K2[4], K3[4], K4[4], K5[4], K6[4], K7[4], Kb[4];
#define LD4(dst, ptr) { _Pragma("unroll") for (int u = 0; u < 4; ++u) dst[u] = ldnt(ptr + u * USTR); ptr += TSTR; __builtin_amdgcn_sched_barrier(0); }
  LD4(K0, kp)
  {
    float v[8];
#pragma unroll
    for (int j = 0; j < 4; ++j) { v[2 * j] = lo16(xv[j]); v[2 * j + 1] = hi16(xv[j]); }
    asm volatile("" : "+v"(v[0]), "+v"(v[1]), "+v"(v[2]), "+v"(v[3]), "+v"(v[4]), "+v"(v[5]), "+v"(v[6]), "+v"(v[7]));
    STAMP(10);
    float s = 0.f;
#pragma unroll
    for (int j = 0; j < 8; ++j) s += v[j];
    const float mean = wave_sum(s) * (1.f / D);
    float q = 0.f;
#pragma unroll
    for (int j = 0; j < 8; ++j) { const float d = v[j] - mean; q = fmaf(d, d, q); }
    if (lane >= 56) q = 0.f;
    const float var = rbf(wave_sum(q) * (1.f / D));
    const float mb = rbf(mean);
    const float inv = 1.f / rbf(sqrtf(rbf(var + 1e-5f)));
    if (lane < 56) {
#pragma unroll
      for (int j = 0; j < 4; ++j) {
        const unsigned lo = f2bf(rbf(v[2 * j] - mb) * inv), hi = f2bf(rbf(v[2 * j + 1] - mb) * inv);
        lds_u[L_XNP + (lane * 4 + j) * 8 + lane * 4 + w] = lo | (hi << 16);
      }
    }
  }
  STAMP(14);
  lds_barrier();
  STAMP(1);
  if (w < 7) {
    float x[16];
#pragma unroll
    for (int v = 0; v < 16; ++v) x[v] = 0.f;
#pragma unroll
    for (int j = 0; j < 4; ++j) {
      const int kq = sp * 56 + w * 2 + (lane >> 5) + 14 * j;
      const unsigned* xr = lds_u + L_XNP + kq * 8 + (kq >> 2) * 4;
      const v4u xa = *(const v4u*)xr, xb = *(const v4u*)(xr + 4);
      const unsigned pa = __builtin_amdgcn_perm(wh[j], wl[j], 0x05040100u), pb = __builtin_amdgcn_perm(wh[j], wl[j], 0x07060302u);
#pragma unroll
      for (int bb = 0; bb < 4; ++bb) { x[bb] = dot2(pa, xa[bb], x[bb]); x[4 + bb] = dot2(pa, xb[bb], x[4 + bb]); x[8 + bb] = dot2(pb, xa[bb], x[8 + bb]); x[12 + bb] = dot2(pb, xb[bb], x[12 + bb]); }
    }
#pragma unroll
    for (int u = 0; u < 8; ++u) wreg[u * 64 + 2 * c + (lane >> 5)] = tr32(x[u], x[u + 8]);
  }
  STAMP(11);
  asm volatile("s_waitcnt vmcnt(4)" ::: "memory");
  LD4(K1, kp)
  lds_barrier();
  STAMP(12);
  const unsigned T = (epoch + 1u) | 0x80000000u;
  {
    float v0 = 0.f;
#pragma unroll
    for (int wv = 0; wv < 7; ++wv) v0 += lds[wv * L_WREG + w * 64 + lane];
    st_gran(qg + ((kvh * 8 + w) * 4 + sp) * 512 + b * 64 + lane, T, __float_as_uint(v0));
  }
  STAMP(2);
  if (w == 0) {
    const unsigned qoff = WS_QG + (unsigned)(kvh * 8 + b) * 16384 + lane * 16;
    v4u g[16];
    for (unsigned n = 0; n < SPIN_CAP; ++n) {
      asm volatile("" ::: "memory");
#pragma unroll
      for (int i = 0; i < 16; ++i) g[i] = __builtin_amdgcn_raw_buffer_load_b128(rs, qoff + i * 1024, 0, 16);
      bool ok = true;
#pragma unroll
      for (int i = 0; i < 16; ++i) ok &= (g[i].y == T) & (g[i].w == T);
      if (__all(ok)) break;
    }
#pragma unroll
    for (int cc = 0; cc < 4; ++cc) {
      const float q0 = ((__uint_as_float(g[cc].x) + __uint_as_float(g[4 + cc].x)) + __uint_as_float(g[8 + cc].x)) + __uint_as_float(g[12 + cc].x);
      const float q1 = ((__uint_as_float(g[cc].z) + __uint_as_float(g[4 + cc].z)) + __uint_as_float(g[8 + cc].z)) + __uint_as_float(g[12 + cc].z);
      lds_u[L_Q + cc * 64 + lane] = f2bf(q0) | (f2bf(q1) << 16);
    }
    if (lane == 0) __hip_atomic_store(lds_u + L_CTR, 1u, __ATOMIC_RELAXED, __HIP_MEMORY_SCOPE_WORKGROUP);
  }
#define PACE() { if (__builtin_amdgcn_readfirstlane(__hip_atomic_load(lds_u + L_CTR, __ATOMIC_RELAXED, __HIP_MEMORY_SCOPE_WORKGROUP)) == 0u) asm volatile("s_waitcnt vmcnt(0)" ::: "memory"); }
  PACE() LD4(K2, kp)
  PACE() LD4(K3, kp)
  PACE() LD4(K4, kp)
  PACE() LD4(K5, kp)
  PACE() LD4(K6, kp)
  PACE() LD4(K7, kp)
  lds_barrier();
  STAMP(3);
  v4u qv[2];
  qv[0] = *(const v4u*)(lds_u + L_Q + c * 4);
  qv[1] = *(const v4u*)(lds_u + L_Q + 128 + c * 4);

  constexpr float C2 = 0.0625f * 1.4426950408889634f;
  float mx = -INFINITY;
  int slot = w;
  const int sidx = (lane >> 5) * 8 + ((lane >> 1) & 7);
  const bool swr = (lane & 0x11) == 0;
#define TR_STEP(HALF, CTRL, BIT) _Pragma("unroll") for (int u = 0; u < (HALF); ++u) { \
        const float keep = (lane & (BIT)) ? x[u + (HALF)] : x[u]; const float send = (lane & (BIT)) ? x[u] : x[u + (HALF)]; \
        x[u] = keep + dpp<CTRL>(send); }
#define QK(KX) { float x[8]; \
      _Pragma("unroll") for (int h = 0; h < 2; ++h) \
        _Pragma("unroll") for (int i = 0; i < 4; ++i) { float d = 0.f; \
          _Pragma("unroll") for (int k = 0; k < 4; ++k) d = dot2(KX[i][k], qv[h][k], d); \
          x[h * 4 + i] = d; } \
      TR_STEP(4, XOR8, 8) TR_STEP(2, XOR7, 4) TR_STEP(1, XOR2, 2) \
      float s = x[0]; s += dpp<XOR1>(s); s = x16_sum(s); \
      s = rbf(s) * C2; \
      mx = fmaxf(mx, s); \
      if (swr) lds[L_SC + slot * 16 + sidx] = s; \
      slot += 8; \
      __builtin_amdgcn_sched_barrier(0); TST(); }
#define SEGA(N) _Pragma("unroll 1") for (int t = 0; t < (N); ++t) { LD4(Kb, kp) QK(Kb) }
#define DMA2() { glds16(dsrc, dlds); glds16(dsrc + dstr, dlds + 1024); dsrc += 2 * dstr; dlds += 2048; }
#define SEGAD(N) _Pragma("unroll 1") for (int t = 0; t < (N); ++t) { DMA2() LD4(Kb, kp) QK(Kb) }
  QK(K0) QK(K1) QK(K2) QK(K3)
  LD4(Kb, kp)
  QK(K4) QK(K5) QK(K6) QK(K7)
  QK(Kb)
  STAMP(16);
  const int l55 = lane < 56 ? lane : 55;
  {
    const char* dsrc; size_t dstr; unsigned dlds = (unsigned)w * (L_WREG * 4);
    if (w < 7) { dsrc = (const char*)(P.w2 + (size_t)(56 * part + 8 * w) * D + l55 * 8); dstr = D * 2; }
    else { dsrc = (const char*)(P.w1 + (size_t)(56 * f1kq + (lane >> 3)) * F + (has_f1 ? f1cb : 0) * 64 + (lane & 7) * 8); dstr = has_f1 ? (size_t)8 * F * 2 : 0; }
    SEGAD(3)
    if (w == 7) dstr = 0;
    SEGAD(1)
  }
  STAMP(17);
  LD4(Kb, kp)
  if (w == 7 && has_f1) {
    float x[16];
#pragma unroll
    for (int v = 0; v < 16; ++v) x[v] = 0.f;
#pragma unroll 2
    for (int j = 0; j < 14; ++j) {
      const int kl = (lane >> 5) + 2 * j, kq = f1kq * 28 + kl;
      const unsigned* xr = lds_u + L_XNP + kq * 8 + (kq >> 2) * 4;
      const v4u xa = *(const v4u*)xr, xb = *(const v4u*)(xr + 4);
      const unsigned a0 = wreg_u[(2 * kl) * 32 + c], a1 = wreg_u[(2 * kl + 1) * 32 + c];
      const unsigned pa = __builtin_amdgcn_perm(a1, a0, 0x05040100u), pb = __builtin_amdgcn_perm(a1, a0, 0x07060302u);
#pragma unroll
      for (int bb = 0; bb < 4; ++bb) { x[bb] = dot2(pa, xa[bb], x[bb]); x[4 + bb] = dot2(pa, xb[bb], x[4 + bb]); x[8 + bb] = dot2(pb, xa[bb], x[8 + bb]); x[12 + bb] = dot2(pb, xb[bb], x[12 + bb]); }
    }
    GAS u64* hd = hg + (size_t)f1kq * 1792 + f1cb * 64 + 2 * c + (lane >> 5);
#pragma unroll
    for (int u = 0; u < 8; ++u) st_gran(hd + (size_t)u * (8 * 1792), T, __float_as_uint(tr32(x[u], x[u + 8])));
  }
  QK(Kb)
  SEGA(5)
  STAMP(18);
  {
    GAS u64* rgp[4]; u64 rg[4];
    const int hcol = 56 * part + 8 * (w < 7 ? w : 0) + (lane & 7);
    {
      const int pu = (hcol >> 6) * 8 + (lane >> 3), pbid = (pu / 7) * 8 + pu % 7;
#pragma unroll
      for (int j = 0; j < 4; ++j) {
        const int i = w < 7 ? pbid : lane * 4 + j;
        rgp[j] = (w < 7 && j == 0) ? hg + (size_t)(b * 8 + (lane >> 3)) * 1792 + hcol : qg + (size_t)((((i >> 2) & 7) * 8) * 4 + (i & 3)) * 512 + (i >> 5) * 64;
        rg[j] = ld_gran(rgp[j]);
      }
    }
    const float b1v = bf2f(P.b1[hcol]);
    SEGA(1)
    LD4(Kb, kp)
    asm volatile("s_waitcnt vmcnt(4)" : "+v"(rg[0]), "+v"(rg[1]), "+v"(rg[2]), "+v"(rg[3]));
    for (unsigned n = 0; n < SPIN_CAP; ++n) {
      if (__all((unsigned)(rg[0] >> 32) == T && (unsigned)(rg[1] >> 32) == T && (unsigned)(rg[2] >> 32) == T && (unsigned)(rg[3] >> 32) == T)) break;
      __builtin_amdgcn_s_sleep(1);
#pragma unroll
      for (int j = 0; j < 4; ++j) rg[j] = ld_gran(rgp[j]);
    }
    if (w < 7) {
      float hv = __uint_as_float((unsigned)rg[0]);
      hv += dpp<XOR8>(hv); hv = x16_sum(hv); hv = x32_sum(hv);
      const float t = rbf(rbf(hv) + b1v);
      const float u = 0.7978845608028654f * (t + 0.044715f * t * t * t);
      const float th = 1.f - 2.f / (__expf(2.f * u) + 1.f);
      const unsigned hb = f2bf(0.5f * t * (1.f + th));
      const unsigned hpk = hb | (__builtin_bit_cast(unsigned, dpp<XOR1>(__builtin_bit_cast(float, hb))) << 16);
      float ya[8];
#pragma unroll
      for (int j = 0; j < 8; ++j) ya[j] = 0.f;
#pragma unroll
      for (int i = 0; i < 4; ++i) {
        const unsigned hp = __builtin_amdgcn_readlane(hpk, 2 * i);
        const v4u r0 = *(const v4u*)(wreg_u + (2 * i) * 256 + lane * 4), r1 = *(const v4u*)(wreg_u + (2 * i + 1) * 256 + lane * 4);
#pragma unroll
        for (int q = 0; q < 4; ++q) {
          ya[2 * q] = dot2(__builtin_amdgcn_perm(r1[q], r0[q], 0x05040100u), hp, ya[2 * q]);
          ya[2 * q + 1] = dot2(__builtin_amdgcn_perm(r1[q], r0[q], 0x07060302u), hp, ya[2 * q + 1]);
        }
      }
      if (lane < 56) { *(f32x4*)(lds + L_Y + w * 448 + lane * 8) = f32x4{ya[0], ya[1], ya[2], ya[3]}; *(f32x4*)(lds + L_Y + w * 448 + lane * 8 + 4) = f32x4{ya[4], ya[5], ya[6], ya[7]}; }
    } else {
      if (bid == 0 && lane == 0) __hip_atomic_store((GAS unsigned*)(ws + WS_EPOCH), epoch + 1u, RLX_AGENT);
    }
    asm volatile("s_waitcnt lgkmcnt(0)" ::: "memory");
  }
  QK(Kb)
  STAMP(19);
  SEGA(GS - 21)
  STAMP(20);
  v4u VA[4], VB[4], Vb[4];
  const GAS v4u* vp = vbase + (size_t)sp * GS * TSTR + w * 64;
  LD4(VA, vp) LD4(VB, vp)
  mx = fmaxf(mx, dpp<XOR2>(mx)); mx = fmaxf(mx, dpp<XOR7>(mx)); mx = x32_max(mx);
  const float M = mx;
  STAMP(21);
  float acc[2][8];
#pragma unroll
  for (int h = 0; h < 2; ++h)
#pragma unroll
    for (int d = 0; d < 8; ++d) acc[h][d] = 0.f;
  float l = 0.f;
#define PV1(VX, I) { float vf[8]; _Pragma("unroll") for (int k = 0; k < 4; ++k) { vf[2 * k] = lo16(VX[I][k]); vf[2 * k + 1] = hi16(VX[I][k]); } \
        const float p0 = dpp<0x150 + 2 * I>(p), p1 = dpp<0x158 + 2 * I>(p); \
        _Pragma("unroll") for (int d = 0; d < 8; ++d) { acc[0][d] = fmaf(p0, vf[d], acc[0][d]); acc[1][d] = fmaf(p1, vf[d], acc[1][d]); } }
#define PV(VX, SL) { const float p = __builtin_amdgcn_exp2f(lds[L_SC + (SL) * 16 + sidx] - M); l += p; \
        PV1(VX, 0) PV1(VX, 1) PV1(VX, 2) PV1(VX, 3) __builtin_amdgcn_sched_barrier(0); TST(); }
#define BTILE() { LD4(Vb, vp) PV(Vb, cur) cur += 8; }
  int cur = 16 + w;
  PV(VA, w) PV(VB, 8 + w)
  v4u wor[16];
  {
    const char* dsrc = (const char*)(P.wo + (size_t)((kvh * 2 + (sp >> 1)) * 256 + (sp & 1) * 128 + 16 * w) * D + l55 * 8);
    const size_t dstr = D * 2;
#pragma unroll
    for (int hf = 0; hf < 2; ++hf) {
      unsigned dlds = (unsigned)w * (L_WREG * 4);
#pragma unroll 1
      for (int t = 0; t < 4; ++t) { DMA2() BTILE() }
      asm volatile("s_waitcnt vmcnt(0)" ::: "memory");
#pragma unroll
      for (int i = 0; i < 4; ++i) {
        const v4u r0 = *(const v4u*)(wreg_u + (2 * i) * 256 + lane * 4), r1 = *(const v4u*)(wreg_u + (2 * i + 1) * 256 + lane * 4);
#pragma unroll
        for (int q = 0; q < 4; ++q) {
          wor[8 * hf + 2 * i][q] = __builtin_amdgcn_perm(r1[q], r0[q], 0x05040100u);
          wor[8 * hf + 2 * i + 1][q] = __builtin_amdgcn_perm(r1[q], r0[q], 0x07060302u);
        }
      }
#pragma unroll
      for (int r = 0; r < 8; ++r) asm volatile("" : "+v"(wor[8 * hf + r]));
      asm volatile("s_waitcnt lgkmcnt(0)" ::: "memory");
    }
  }
  float xin = 0.f, b2v = 0.f;
  if (part < 28 && tid < 16) { xin = bf2f(P.x[b * D + part * 16 + tid]); b2v = bf2f(P.b2[part * 16 + tid]); }
  STAMP(22);
#pragma unroll 1
  for (int t = 0; t < GS - 10; ++t) BTILE()
  STAMP(4);
#pragma unroll
  for (int h = 0; h < 2; ++h)
#pragma unroll
    for (int d = 0; d < 8; ++d) acc[h][d] = x32_sum(acc[h][d]);
  l += dpp<XOR2>(l); l += dpp<XOR7>(l); l = x32_sum(l);
  if (lane < 32) {
#pragma unroll
    for (int h = 0; h < 2; ++h) {
      *(f32x4*)(wreg + h * 260 + 4 + c * 8) = f32x4{acc[h][0], acc[h][1], acc[h][2], acc[h][3]};
      *(f32x4*)(wreg + h * 260 + 4 + c * 8 + 4) = f32x4{acc[h][4], acc[h][5], acc[h][6], acc[h][7]};
    }
    if ((lane & 7) == 0 && lane < 16) { wreg[(lane >> 3) * 260] = M; wreg[(lane >> 3) * 260 + 1] = l; }
  }
  lds_barrier();
  {
    const int h = tid >> 8, d = tid & 255;
    const float* mrg = lds + h * 260;
    float Mx = -INFINITY;
#pragma unroll
    for (int ww = 0; ww < NW; ++ww) Mx = fmaxf(Mx, mrg[ww * L_WREG]);
    float L = 0.f, o = 0.f;
#pragma unroll
    for (int ww = 0; ww < NW; ++ww) { const float wt = __builtin_amdgcn_exp2f(mrg[ww * L_WREG] - Mx); L = fmaf(wt, mrg[ww * L_WREG + 1], L); o = fmaf(wt, mrg[ww * L_WREG + 4 + d], o); }
    GAS u64* slotp = pg + (size_t)bid * PG_STRIDE;
    st_gran(slotp + h * 256 + d, T, __float_as_uint(o));
    if (d < 2) st_gran(slotp + 512 + 2 * h + d, T, __float_as_uint(d == 0 ? Mx : L));
  }
  STAMP(5);
  {
    const int hs = sp >> 1, pj = lane >> 4, j = lane & 15;
    GAS u64* slot = pg + (size_t)(pair * 4 + pj) * PG_STRIDE;
    GAS u64* ga = slot + hs * 256 + (sp & 1) * 128 + 16 * w + j;
    GAS u64* gm = slot + 512 + 2 * hs + (j & 1);
    u64 a, ml;
#pragma unroll 1
    for (unsigned n = 0; n < SPIN_CAP; ++n) {
      a = ld_gran(ga); ml = ld_gran(gm);
      if (__all((unsigned)(a >> 32) == T && (unsigned)(ml >> 32) == T)) break;
    }
    STAMP(6);
    const float mlv = __uint_as_float((unsigned)ml), oth = dpp<XOR1>(mlv);
    const float Mp = (j & 1) ? oth : mlv, Lp = (j & 1) ? mlv : oth;
    const float M = x32_max(x16_max(Mp));
    const float wt = __builtin_amdgcn_exp2f(Mp - M);
    const float L = x32_sum(x16_sum(wt * Lp));
    const float o = x32_sum(x16_sum(wt * __uint_as_float((unsigned)a)));
    const unsigned ob = f2bf(o / L);
    const unsigned pk = ob | (__builtin_bit_cast(unsigned, dpp<XOR1>(__builtin_bit_cast(float, ob))) << 16);
    float sa[8];
#pragma unroll
    for (int q = 0; q < 8; ++q) sa[q] = 0.f;
#pragma unroll
    for (int i = 0; i < 8; ++i) {
      const unsigned op = __builtin_amdgcn_readlane(pk, 2 * i);
#pragma unroll
      for (int q = 0; q < 4; ++q) { sa[2 * q] = dot2(wor[2 * i][q], op, sa[2 * q]); sa[2 * q + 1] = dot2(wor[2 * i + 1][q], op, sa[2 * q + 1]); }
    }
    if (lane < 56) { *(f32x4*)(wreg + 1024 + lane * 8) = f32x4{sa[0], sa[1], sa[2], sa[3]}; *(f32x4*)(wreg + 1024 + lane * 8 + 4) = f32x4{sa[4], sa[5], sa[6], sa[7]}; }
  }
  lds_barrier();
  if (tid < 448) {
    float s = 0.f, y = 0.f;
#pragma unroll
    for (int ww = 0; ww < 8; ++ww) s += lds[ww * L_WREG + 1024 + tid];
#pragma unroll
    for (int ww = 0; ww < 7; ++ww) y += lds[L_Y + ww * 448 + tid];
    __builtin_amdgcn_raw_buffer_store_b128(v4u{__float_as_uint(s), T, __float_as_uint(y), T}, rs, WS_SG + (unsigned)(b * 32 + part) * (448 * 16) + tid * 16, 0, 16);
  }
  STAMP(7);
  if (part < 28) {
    const int pp = tid >> 4, col = tid & 15;
    const unsigned off = WS_SG + (unsigned)(b * 32 + pp) * (448 * 16) + (part * 16 + col) * 16;
    v4u g;
#pragma unroll 1
    for (unsigned n = 0; n < SPIN_CAP; ++n) {
      asm volatile("" ::: "memory");
      g = __builtin_amdgcn_raw_buffer_load_b128(rs, off, 0, 16);
      if (__all(g.y == T && g.w == T)) break;
    }
    STAMP(8);
    const float s = x32_sum(x16_sum(__uint_as_float(g.x))), y = x32_sum(x16_sum(__uint_as_float(g.z)));
    if (lane < 16) *(f32x2*)(wreg + 1536 + lane * 2) = f32x2{s, y};
    lds_barrier();
    if (tid < 16) {
      float st = 0.f, yt = 0.f;
#pragma unroll
      for (int ww = 0; ww < 8; ++ww) { st += lds[ww * L_WREG + 1536 + tid * 2]; yt += lds[ww * L_WREG + 1536 + tid * 2 + 1]; }
      st = rbf(st);
      yt = rbf(rbf(yt) + b2v);
      const float g2 = rbf(yt / (1.f + __expf(-yt)));
      P.out[b * D + part * 16 + tid] = (unsigned short)f2bf(xin + rbf(st * g2));
    }
  }
  STAMP(9);
}

extern "C" void kernel_launch(void* const* d_in, const int* in_sizes, int n_in, void* d_out, int out_size, void* d_ws, size_t ws_size,
                              hipStream_t stream) {
  Params P;
  P.x = (const unsigned short*)d_in[0];
  P.kc = (const unsigned short*)d_in[1];
  P.vc = (const unsigned short*)d_in[2];
  P.wq = (const unsigned short*)d_in[3];
  P.wo = (const unsigned short*)d_in[4];
  P.w1 = (const unsigned short*)d_in[6];
  P.b1 = (const unsigned short*)d_in[7];
  P.w2 = (const unsigned short*)d_in[8];
  P.b2 = (const unsigned short*)d_in[9];
  P.out = (unsigned short*)d_out;
  P.ws = (unsigned char*)d_ws;
  (void)hipFuncSetAttribute((const void*)fused_kernel, hipFuncAttributeMaxDynamicSharedMemorySize, LDS_BYTES);
  fused_kernel<<<NWG, NTHR, LDS_BYTES, stream>>>(P);
}
```

```cpp
#include <hip/hip_runtime.h>
#include <hip/hip_bf16.h>
#include <stdint.h>

typedef unsigned v4u __attribute__((ext_vector_type(4)));
typedef float f32x4 __attribute__((ext_vector_type(4)));
typedef float f32x2 __attribute__((ext_vector_type(2)));
typedef __attribute__((ext_vector_type(2))) __bf16 bf16x2;
typedef unsigned long long u64;
#define GAS __attribute__((address_space(1)))
#define RLX_AGENT __ATOMIC_RELAXED, __HIP_MEMORY_SCOPE_AGENT

constexpr int B = 8, D = 448, HKV = 8, SK = 8192, DH = 256, F = 1792;
constexpr int NWG = 256, NW = 8, NTHR = 512;
constexpr int GS = 32;
constexpr unsigned SPIN_CAP = 1u << 17;
constexpr unsigned OOB = 0x80000000u;

constexpr unsigned WS_EPOCH = 0;
constexpr unsigned WS_QG = 4096;
constexpr unsigned WS_HG = WS_QG + 8 * 8 * 4 * 512 * 8;
constexpr unsigned WS_PG = WS_HG + 8 * 8 * 1792 * 8;
constexpr unsigned PG_STRIDE = 576;
constexpr unsigned WS_SG = WS_PG + 256 * PG_STRIDE * 8;
constexpr unsigned WS_END = WS_SG + 8 * 32 * 448 * 16;

__device__ __forceinline__ float bf2f(unsigned short b) { return __builtin_bit_cast(float, (unsigned)b << 16); }
__device__ __forceinline__ float rbf(float f) { return (float)(__bf16)f; }
__device__ __forceinline__ unsigned f2bf(float f) { return (unsigned)__builtin_bit_cast(unsigned short, (__bf16)f); }
__device__ __forceinline__ float lo16(unsigned u) { return __builtin_bit_cast(float, u << 16); }
__device__ __forceinline__ float hi16(unsigned u) { return __builtin_bit_cast(float, u & 0xffff0000u); }
__device__ __forceinline__ float dot2(unsigned k, unsigned q, float acc) {
  return __builtin_amdgcn_fdot2_f32_bf16(__builtin_bit_cast(bf16x2, k), __builtin_bit_cast(bf16x2, q), acc, false);
}
template <int CTRL> __device__ __forceinline__ float dpp(float x) {
  return __builtin_bit_cast(float, __builtin_amdgcn_mov_dpp(__builtin_bit_cast(int, x), CTRL, 0xf, 0xf, true));
}
constexpr int XOR1 = 0xB1, XOR2 = 0x4E, XOR7 = 0x141, XOR8 = 0x128;
__device__ __forceinline__ float x16_sum(float x) {
  auto s = __builtin_amdgcn_permlane16_swap(__float_as_uint(x), __float_as_uint(x), false, false);
  return __uint_as_float(s[0]) + __uint_as_float(s[1]);
}
__device__ __forceinline__ float x32_sum(float x) {
  auto s = __builtin_amdgcn_permlane32_swap(__float_as_uint(x), __float_as_uint(x), false, false);
  return __uint_as_float(s[0]) + __uint_as_float(s[1]);
}
__device__ __forceinline__ float x16_max(float x) {
  auto s = __builtin_amdgcn_permlane16_swap(__float_as_uint(x), __float_as_uint(x), false, false);
  return fmaxf(__uint_as_float(s[0]), __uint_as_float(s[1]));
}
__device__ __forceinline__ float x32_max(float x) {
  auto s = __builtin_amdgcn_permlane32_swap(__float_as_uint(x), __float_as_uint(x), false, false);
  return fmaxf(__uint_as_float(s[0]), __uint_as_float(s[1]));
}
__device__ __forceinline__ float tr32(float a, float b) {
  auto s = __builtin_amdgcn_permlane32_swap(__float_as_uint(a), __float_as_uint(b), false, false);
  return __uint_as_float(s[0]) + __uint_as_float(s[1]);
}
__device__ __forceinline__ float wave_sum(float x) {
  x += dpp<XOR1>(x); x += dpp<XOR2>(x); x += dpp<XOR7>(x); x += dpp<XOR8>(x); x = x16_sum(x); return x32_sum(x);
}
__device__ __forceinline__ v4u ldnt(const GAS v4u* p) { return __builtin_nontemporal_load(p); }
__device__ __forceinline__ void st_gran(GAS u64* g, unsigned tag, unsigned val) { __hip_atomic_store(g, ((u64)tag << 32) | val, RLX_AGENT); }
__device__ __forceinline__ u64 ld_gran(GAS u64* g) { return __hip_atomic_load(g, RLX_AGENT); }
__device__ __forceinline__ void lds_barrier() { asm volatile("s_waitcnt lgkmcnt(0)" ::: "memory"); __builtin_amdgcn_s_barrier(); asm volatile("" ::: "memory"); }
__device__ __forceinline__ __amdgpu_buffer_rsrc_t mk_rsrc(const void* p, unsigned bytes) { return __builtin_amdgcn_make_buffer_rsrc((void*)p, 0, bytes, 0x00020000); }
__device__ __forceinline__ void glds16(const void* gsrc, unsigned lds_dst) {
  unsigned keep;
  asm volatile("s_mov_b32 %0, m0\n\ts_mov_b32 m0, %2\n\ts_nop 0\n\tglobal_load_lds_dwordx4 %1, off nt\n\ts_mov_b32 m0, %0" : "=&s"(keep) : "v"(gsrc), "s"(lds_dst) : "memory");
}

struct Params {
  const unsigned short *x, *kc, *vc, *wq, *wo, *w1, *b1, *w2, *b2;
  unsigned short* out;
  unsigned char* ws;
};

constexpr int L_WREG = 2048;
constexpr int L_XNP = 8 * L_WREG;
constexpr int L_Q = L_XNP + 2016;
constexpr int L_Y = L_Q + 256;
constexpr int L_SC = L_Y + 7 * 448;
constexpr int NSLOT = 8 * GS;
constexpr int L_RED = L_SC + NSLOT * 16;
constexpr int L_CTR = L_RED + 7 * 512;
constexpr int L_END = L_CTR + 4;
constexpr int LDS_BYTES = L_END * 4;
static_assert(LDS_BYTES <= 160 * 1024 && LDS_BYTES > 80 * 1024 && L_Y % 4 == 0 && L_Q % 4 == 0 && L_SC % 4 == 0, "");

__global__ void __launch_bounds__(NTHR, 2) fused_kernel(Params P) {
  extern __shared__ __attribute__((aligned(16))) float lds[];
  unsigned* lds_u = (unsigned*)lds;
  const int tid = threadIdx.x, lane = tid & 63, w = __builtin_amdgcn_readfirstlane(tid >> 6);
  const int bid = blockIdx.x;
  const int pair = bid >> 2, sp = bid & 3, b = pair >> 3, kvh = pair & 7;
  const int part = kvh * 4 + sp;
  const int c = lane & 31;
  GAS unsigned char* ws = (GAS unsigned char*)P.ws;
  GAS u64* qg = (GAS u64*)(ws + WS_QG);
  GAS u64* hg = (GAS u64*)(ws + WS_HG);
  GAS u64* pg = (GAS u64*)(ws + WS_PG);
  const __amdgpu_buffer_rsrc_t rs = mk_rsrc(P.ws, WS_END);
  float* wreg = lds + w * L_WREG;
  unsigned* wreg_u = lds_u + w * L_WREG;
  const bool has_f1 = (bid & 7) != 7;
  const int f1u = (bid >> 3) * 7 + (bid & 7), f1cb = f1u >> 3, f1kq = f1u & 7;
  const unsigned epoch = __hip_atomic_load((GAS unsigned*)(ws + WS_EPOCH), RLX_AGENT);
  const unsigned l56 = lane < 56 ? lane * 16 : OOB;
  const v4u xv = __builtin_amdgcn_raw_buffer_load_b128(mk_rsrc(P.x, B * D * 2), l56 + w * (D * 2), 0, 2);
  unsigned wl[4], wh[4];
  {
    const __amdgpu_buffer_rsrc_t r = mk_rsrc(P.wq, D * 4096 * 2);
    const unsigned o0 = tid < 448 ? (unsigned)((sp * 112 + 2 * (w * 2 + (lane >> 5))) * 4096 + kvh * 512 + b * 64) * 2 + c * 4 : OOB;
#pragma unroll
    for (int j = 0; j < 4; ++j) {
      wl[j] = __builtin_amdgcn_raw_buffer_load_b32(r, o0 + (unsigned)(28 * j) * 8192, 0, 2);
      wh[j] = __builtin_amdgcn_raw_buffer_load_b32(r, o0 + (unsigned)(28 * j + 1) * 8192, 0, 2);
    }
  }
  {
    const v4u z = {0u, 0u, 0u, 0u};
    if (tid < (int)PG_STRIDE / 2) __builtin_amdgcn_raw_buffer_store_b128(z, rs, WS_PG + (unsigned)bid * (PG_STRIDE * 8) + tid * 16, 0, 16);
    if (tid < 448) __builtin_amdgcn_raw_buffer_store_b128(z, rs, WS_SG + (unsigned)(b * 32 + part) * (448 * 16) + tid * 16, 0, 16);
    if (has_f1 && tid < 256) __builtin_amdgcn_raw_buffer_store_b128(z, rs, WS_HG + (unsigned)((((tid >> 5) * 8 + f1kq) * 1792 + f1cb * 64) * 8) + (tid & 31) * 16, 0, 16);
    if (tid == 0) lds_u[L_CTR] = 0u;
  }
  __builtin_amdgcn_sched_barrier(0);
  __builtin_amdgcn_s_barrier();
  const size_t base = (size_t)pair * SK * DH;
  constexpr int USTR = NW * 64, TSTR = NW * 256;
  const GAS v4u* kbase = (const GAS v4u*)(P.kc + base) + lane;
  const GAS v4u* vbase = (const GAS v4u*)(P.vc + base) + lane;
  const GAS v4u* kp = kbase + (size_t)sp * GS * TSTR + w * 64;
  v4u K0[4], K1[4], K2[4], K3[4], K4[4], K5[4], K6[4], K7[4], K8[4], K9[4], Kb[4];
#define LD4(dst, ptr) { _Pragma("unroll") for (int u = 0; u < 4; ++u) dst[u] = ldnt(ptr + u * USTR); ptr += TSTR; __builtin_amdgcn_sched_barrier(0); }
  LD4(K0, kp)
  {
    float v[8];
#pragma unroll
    for (int j = 0; j < 4; ++j) { v[2 * j] = lo16(xv[j]); v[2 * j + 1] = hi16(xv[j]); }
    asm volatile("" : "+v"(v[0]), "+v"(v[1]), "+v"(v[2]), "+v"(v[3]), "+v"(v[4]), "+v"(v[5]), "+v"(v[6]), "+v"(v[7]));
    float s = 0.f;
#pragma unroll
    for (int j = 0; j < 8; ++j) s += v[j];
    const float mean = wave_sum(s) * (1.f / D);
    float q = 0.f;
#pragma unroll
    for (int j = 0; j < 8; ++j) { const float d = v[j] - mean; q = fmaf(d, d, q); }
    if (lane >= 56) q = 0.f;
    const float var = rbf(wave_sum(q) * (1.f / D));
    const float mb = rbf(mean);
    const float inv = 1.f / rbf(sqrtf(rbf(var + 1e-5f)));
    if (lane < 56) {
#pragma unroll
      for (int j = 0; j < 4; ++j) {
        const unsigned lo = f2bf(rbf(v[2 * j] - mb) * inv), hi = f2bf(rbf(v[2 * j + 1] - mb) * inv);
        lds_u[L_XNP + (lane * 4 + j) * 8 + lane * 4 + w] = lo | (hi << 16);
      }
    }
  }
  lds_barrier();
  if (w < 7) {
    float x[16];
#pragma unroll
    for (int v = 0; v < 16; ++v) x[v] = 0.f;
#pragma unroll
    for (int j = 0; j < 4; ++j) {
      const int kq = sp * 56 + w * 2 + (lane >> 5) + 14 * j;
      const unsigned* xr = lds_u + L_XNP + kq * 8 + (kq >> 2) * 4;
      const v4u xa = *(const v4u*)xr, xb = *(const v4u*)(xr + 4);
      const unsigned pa = __builtin_amdgcn_perm(wh[j], wl[j], 0x05040100u), pb = __builtin_amdgcn_perm(wh[j], wl[j], 0x07060302u);
#pragma unroll
      for (int bb = 0; bb < 4; ++bb) { x[bb] = dot2(pa, xa[bb], x[bb]); x[4 + bb] = dot2(pa, xb[bb], x[4 + bb]); x[8 + bb] = dot2(pb, xa[bb], x[8 + bb]); x[12 + bb] = dot2(pb, xb[bb], x[12 + bb]); }
    }
#pragma unroll
    for (int u = 0; u < 8; ++u) lds[L_RED + w * 512 + u * 64 + 2 * c + (lane >> 5)] = tr32(x[u], x[u + 8]);
  }
  asm volatile("s_waitcnt vmcnt(4)" ::: "memory");
  LD4(K1, kp)
  lds_barrier();
  const unsigned T = (epoch + 1u) | 0x80000000u;
  {
    float v0 = 0.f;
#pragma unroll
    for (int wv = 0; wv < 7; ++wv) v0 += lds[L_RED + wv * 512 + w * 64 + lane];
    st_gran(qg + ((kvh * 8 + w) * 4 + sp) * 512 + b * 64 + lane, T, __float_as_uint(v0));
  }
  if (w == 0) {
    const unsigned qoff = WS_QG + (unsigned)(kvh * 8 + b) * 16384 + lane * 16;
    v4u g[16];
    for (unsigned n = 0; n < SPIN_CAP; ++n) {
      asm volatile("" ::: "memory");
#pragma unroll
      for (int i = 0; i < 16; ++i) g[i] = __builtin_amdgcn_raw_buffer_load_b128(rs, qoff + i * 1024, 0, 16);
      bool ok = true;
#pragma unroll
      for (int i = 0; i < 16; ++i) ok &= (g[i].y == T) & (g[i].w == T);
      if (__all(ok)) break;
    }
#pragma unroll
    for (int cc = 0; cc < 4; ++cc) {
      const float q0 = ((__uint_as_float(g[cc].x) + __uint_as_float(g[4 + cc].x)) + __uint_as_float(g[8 + cc].x)) + __uint_as_float(g[12 + cc].x);
      const float q1 = ((__uint_as_float(g[cc].z) + __uint_as_float(g[4 + cc].z)) + __uint_as_float(g[8 + cc].z)) + __uint_as_float(g[12 + cc].z);
      lds_u[L_Q + cc * 64 + lane] = f2bf(q0) | (f2bf(q1) << 16);
    }
    asm volatile("s_waitcnt lgkmcnt(0)" ::: "memory");
    if (lane == 0) __hip_atomic_store(lds_u + L_CTR, 1u, __ATOMIC_RELAXED, __HIP_MEMORY_SCOPE_WORKGROUP);
  }
#define PACE() { if (__builtin_amdgcn_readfirstlane(__hip_atomic_load(lds_u + L_CTR, __ATOMIC_RELAXED, __HIP_MEMORY_SCOPE_WORKGROUP)) == 0u) asm volatile("s_waitcnt vmcnt(0)" ::: "memory"); }
  PACE() LD4(K2, kp)
  PACE() LD4(K3, kp)
  PACE() LD4(K4, kp)
  PACE() LD4(K5, kp)
  PACE() LD4(K6, kp)
  PACE() LD4(K7, kp)
  PACE() LD4(K8, kp)
  PACE() LD4(K9, kp)
  for (unsigned n = 0; n < SPIN_CAP * 16; ++n) {
    if (__builtin_amdgcn_readfirstlane(__hip_atomic_load(lds_u + L_CTR, __ATOMIC_RELAXED, __HIP_MEMORY_SCOPE_WORKGROUP)) != 0u) break;
    __builtin_amdgcn_s_sleep(1);
  }
  asm volatile("" ::: "memory");
  v4u qv[2];
  qv[0] = *(const v4u*)(lds_u + L_Q + c * 4);
  qv[1] = *(const v4u*)(lds_u + L_Q + 128 + c * 4);

  constexpr float C2 = 0.0625f * 1.4426950408889634f;
  float mx = -INFINITY;
  int slot = w;
  const int sidx = (lane >> 5) * 8 + ((lane >> 1) & 7);
  const bool swr = (lane & 0x11) == 0;
#define TR_STEP(HALF, CTRL, BIT) _Pragma("unroll") for (int u = 0; u < (HALF); ++u) { \
        const float keep = (lane & (BIT)) ? x[u + (HALF)] : x[u]; const float send = (lane & (BIT)) ? x[u] : x[u + (HALF)]; \
        x[u] = keep + dpp<CTRL>(send); }
#define QK(KX) { float x[8]; \
      _Pragma("unroll") for (int h = 0; h < 2; ++h) \
        _Pragma("unroll") for (int i = 0; i < 4; ++i) { float d = 0.f; \
          _Pragma("unroll") for (int k = 0; k < 4; ++k) d = dot2(KX[i][k], qv[h][k], d); \
          x[h * 4 + i] = d; } \
      TR_STEP(4, XOR8, 8) TR_STEP(2, XOR7, 4) TR_STEP(1, XOR2, 2) \
      float s = x[0]; s += dpp<XOR1>(s); s = x16_sum(s); \
      s = rbf(s) * C2; \
      mx = fmaxf(mx, s); \
      if (swr) lds[L_SC + slot * 16 + sidx] = s; \
      slot += 8; \
      __builtin_amdgcn_sched_barrier(0); }
#define SEGA(N) _Pragma("unroll 1") for (int t = 0; t < (N); ++t) { LD4(Kb, kp) QK(Kb) }
#define DMA2() { glds16(dsrc, dlds); glds16(dsrc + dstr, dlds + 1024); dsrc += 2 * dstr; dlds += 2048; }
#define SEGAD(N) _Pragma("unroll 1") for (int t = 0; t < (N); ++t) { DMA2() LD4(Kb, kp) QK(Kb) }
  QK(K0) QK(K1) QK(K2) QK(K3) QK(K4)
  LD4(Kb, kp)
  QK(K5) QK(K6) QK(K7) QK(K8) QK(K9)
  QK(Kb)
  const int l55 = lane < 56 ? lane : 55;
  {
    const char* dsrc; size_t dstr; unsigned dlds = (unsigned)w * (L_WREG * 4);
    if (w < 7) { dsrc = (const char*)(P.w2 + (size_t)(56 * part + 8 * w) * D + l55 * 8); dstr = D * 2; }
    else { dsrc = (const char*)(P.w1 + (size_t)(56 * f1kq + (lane >> 3)) * F + (has_f1 ? f1cb : 0) * 64 + (lane & 7) * 8); dstr = has_f1 ? (size_t)8 * F * 2 : 0; }
    SEGAD(3)
    if (w == 7) dstr = 0;
    SEGAD(1)
  }
  LD4(Kb, kp)
  if (w == 7 && has_f1) {
    float x[16];
#pragma unroll
    for (int v = 0; v < 16; ++v) x[v] = 0.f;
#pragma unroll 2
    for (int j = 0; j < 14; ++j) {
      const int kl = (lane >> 5) + 2 * j, kq = f1kq * 28 + kl;
      const unsigned* xr = lds_u + L_XNP + kq * 8 + (kq >> 2) * 4;
      const v4u xa = *(const v4u*)xr, xb = *(const v4u*)(xr + 4);
      const unsigned a0 = wreg_u[(2 * kl) * 32 + c], a1 = wreg_u[(2 * kl + 1) * 32 + c];
      const unsigned pa = __builtin_amdgcn_perm(a1, a0, 0x05040100u), pb = __builtin_amdgcn_perm(a1, a0, 0x07060302u);
#pragma unroll
      for (int bb = 0; bb < 4; ++bb) { x[bb] = dot2(pa, xa[bb], x[bb]); x[4 + bb] = dot2(pa, xb[bb], x[4 + bb]); x[8 + bb] = dot2(pb, xa[bb], x[8 + bb]); x[12 + bb] = dot2(pb, xb[bb], x[12 + bb]); }
    }
    GAS u64* hd = hg + (size_t)f1kq * 1792 + f1cb * 64 + 2 * c + (lane >> 5);
#pragma unroll
    for (int u = 0; u < 8; ++u) st_gran(hd + (size_t)u * (8 * 1792), T, __float_as_uint(tr32(x[u], x[u + 8])));
  }
  QK(Kb)
  SEGA(3)
  {
    GAS u64* rgp[4]; u64 rg[4];
    const int hcol = 56 * part + 8 * (w < 7 ? w : 0) + (lane & 7);
    {
      const int pu = (hcol >> 6) * 8 + (lane >> 3), pbid = (pu / 7) * 8 + pu % 7;
#pragma unroll
      for (int j = 0; j < 4; ++j) {
        const int i = w < 7 ? pbid : lane * 4 + j;
        rgp[j] = (w < 7 && j == 0) ? hg + (size_t)(b * 8 + (lane >> 3)) * 1792 + hcol : qg + (size_t)((((i >> 2) & 7) * 8) * 4 + (i & 3)) * 512 + (i >> 5) * 64;
        rg[j] = ld_gran(rgp[j]);
      }
    }
    const float b1v = bf2f(P.b1[hcol]);
    SEGA(1)
    LD4(Kb, kp)
    asm volatile("s_waitcnt vmcnt(4)" : "+v"(rg[0]), "+v"(rg[1]), "+v"(rg[2]), "+v"(rg[3]));
    for (unsigned n = 0; n < SPIN_CAP; ++n) {
      if (__all((unsigned)(rg[0] >> 32) == T && (unsigned)(rg[1] >> 32) == T && (unsigned)(rg[2] >> 32) == T && (unsigned)(rg[3] >> 32) == T)) break;
      __builtin_amdgcn_s_sleep(1);
#pragma unroll
      for (int j = 0; j < 4; ++j) rg[j] = ld_gran(rgp[j]);
    }
    if (w < 7) {
      float hv = __uint_as_float((unsigned)rg[0]);
      hv += dpp<XOR8>(hv); hv = x16_sum(hv); hv = x32_sum(hv);
      const float t = rbf(rbf(hv) + b1v);
      const float u = 0.7978845608028654f * (t + 0.044715f * t * t * t);
      const float th = 1.f - 2.f / (__expf(2.f * u) + 1.f);
      const unsigned hb = f2bf(0.5f * t * (1.f + th));
      const unsigned hpk = hb | (__builtin_bit_cast(unsigned, dpp<XOR1>(__builtin_bit_cast(float, hb))) << 16);
      float ya[8];
#pragma unroll
      for (int j = 0; j < 8; ++j) ya[j] = 0.f;
#pragma unroll
      for (int i = 0; i < 4; ++i) {
        const unsigned hp = __builtin_amdgcn_readlane(hpk, 2 * i);
        const v4u r0 = *(const v4u*)(wreg_u + (2 * i) * 256 + lane * 4), r1 = *(const v4u*)(wreg_u + (2 * i + 1) * 256 + lane * 4);
#pragma unroll
        for (int q = 0; q < 4; ++q) {
          ya[2 * q] = dot2(__builtin_amdgcn_perm(r1[q], r0[q], 0x05040100u), hp, ya[2 * q]);
          ya[2 * q + 1] = dot2(__builtin_amdgcn_perm(r1[q], r0[q], 0x07060302u), hp, ya[2 * q + 1]);
        }
      }
      if (lane < 56) { *(f32x4*)(lds + L_Y + w * 448 + lane * 8) = f32x4{ya[0], ya[1], ya[2], ya[3]}; *(f32x4*)(lds + L_Y + w * 448 + lane * 8 + 4) = f32x4{ya[4], ya[5], ya[6], ya[7]}; }
    } else {
      if (bid == 0 && lane == 0) __hip_atomic_store((GAS unsigned*)(ws + WS_EPOCH), epoch + 1u, RLX_AGENT);
    }
    asm volatile("s_waitcnt lgkmcnt(0)" ::: "memory");
  }
  QK(Kb)
  SEGA(GS - 21)
  v4u VA[4], VB[4], Vb[4];
  const GAS v4u* vp = vbase + (size_t)sp * GS * TSTR + w * 64;
  LD4(VA, vp) LD4(VB, vp)
  mx = fmaxf(mx, dpp<XOR2>(mx)); mx = fmaxf(mx, dpp<XOR7>(mx)); mx = x32_max(mx);
  const float M = mx;
  float acc[2][8];
#pragma unroll
  for (int h = 0; h < 2; ++h)
#pragma unroll
    for (int d = 0; d < 8; ++d) acc[h][d] = 0.f;
  float l = 0.f;
#define PV1(VX, I) { float vf[8]; _Pragma("unroll") for (int k = 0; k < 4; ++k) { vf[2 * k] = lo16(VX[I][k]); vf[2 * k + 1] = hi16(VX[I][k]); } \
        const float p0 = dpp<0x150 + 2 * I>(p), p1 = dpp<0x158 + 2 * I>(p); \
        _Pragma("unroll") for (int d = 0; d < 8; ++d) { acc[0][d] = fmaf(p0, vf[d], acc[0][d]); acc[1][d] = fmaf(p1, vf[d], acc[1][d]); } }
#define PV(VX, SL) { const float p = __builtin_amdgcn_exp2f(lds[L_SC + (SL) * 16 + sidx] - M); l += p; \
        PV1(VX, 0) PV1(VX, 1) PV1(VX, 2) PV1(VX, 3) __builtin_amdgcn_sched_barrier(0); }
#define BTILE() { LD4(Vb, vp) PV(Vb, cur) cur += 8; }
  int cur = 16 + w;
  PV(VA, w) PV(VB, 8 + w)
  v4u wor[16];
  {
    const char* dsrc = (const char*)(P.wo + (size_t)((kvh * 2 + (sp >> 1)) * 256 + (sp & 1) * 128 + 16 * w) * D + l55 * 8);
    const size_t dstr = D * 2;
#pragma unroll
    for (int hf = 0; hf < 2; ++hf) {
      unsigned dlds = (unsigned)w * (L_WREG * 4);
#pragma unroll 1
      for (int t = 0; t < 4; ++t) { DMA2() BTILE() }
      asm volatile("s_waitcnt vmcnt(0)" ::: "memory");
#pragma unroll
      for (int i = 0; i < 4; ++i) {
        const v4u r0 = *(const v4u*)(wreg_u + (2 * i) * 256 + lane * 4), r1 = *(const v4u*)(wreg_u + (2 * i + 1) * 256 + lane * 4);
#pragma unroll
        for (int q = 0; q < 4; ++q) {
          wor[8 * hf + 2 * i][q] = __builtin_amdgcn_perm(r1[q], r0[q], 0x05040100u);
          wor[8 * hf + 2 * i + 1][q] = __builtin_amdgcn_perm(r1[q], r0[q], 0x07060302u);
        }
      }
#pragma unroll
      for (int r = 0; r < 8; ++r) asm volatile("" : "+v"(wor[8 * hf + r]));
      asm volatile("s_waitcnt lgkmcnt(0)" ::: "memory");
    }
  }
  float xin = 0.f, b2v = 0.f;
  if (part < 28 && tid < 16) { xin = bf2f(P.x[b * D + part * 16 + tid]); b2v = bf2f(P.b2[part * 16 + tid]); }
#pragma unroll 1
  for (int t = 0; t < GS - 10; ++t) BTILE()
#pragma unroll
  for (int h = 0; h < 2; ++h)
#pragma unroll
    for (int d = 0; d < 8; ++d) acc[h][d] = x32_sum(acc[h][d]);
  l += dpp<XOR2>(l); l += dpp<XOR7>(l); l = x32_sum(l);
  if (lane < 32) {
#pragma unroll
    for (int h = 0; h < 2; ++h) {
      *(f32x4*)(wreg + h * 260 + 4 + c * 8) = f32x4{acc[h][0], acc[h][1], acc[h][2], acc[h][3]};
      *(f32x4*)(wreg + h * 260 + 4 + c * 8 + 4) = f32x4{acc[h][4], acc[h][5], acc[h][6], acc[h][7]};
    }
    if ((lane & 7) == 0 && lane < 16) { wreg[(lane >> 3) * 260] = M; wreg[(lane >> 3) * 260 + 1] = l; }
  }
  lds_barrier();
  {
    const int h = tid >> 8, d = tid & 255;
    const float* mrg = lds + h * 260;
    float Mx = -INFINITY;
#pragma unroll
    for (int ww = 0; ww < NW; ++ww) Mx = fmaxf(Mx, mrg[ww * L_WREG]);
    float L = 0.f, o = 0.f;
#pragma unroll
    for (int ww = 0; ww < NW; ++ww) { const float wt = __builtin_amdgcn_exp2f(mrg[ww * L_WREG] - Mx); L = fmaf(wt, mrg[ww * L_WREG + 1], L); o = fmaf(wt, mrg[ww * L_WREG + 4 + d], o); }
    GAS u64* slotp = pg + (size_t)bid * PG_STRIDE;
    st_gran(slotp + h * 256 + d, T, __float_as_uint(o));
    if (d < 2) st_gran(slotp + 512 + 2 * h + d, T, __float_as_uint(d == 0 ? Mx : L));
  }
  {
    const int hs = sp >> 1, pj = lane >> 4, j = lane & 15;
    GAS u64* slot = pg + (size_t)(pair * 4 + pj) * PG_STRIDE;
    GAS u64* ga = slot + hs * 256 + (sp & 1) * 128 + 16 * w + j;
    GAS u64* gm = slot + 512 + 2 * hs + (j & 1);
    u64 a, ml;
#pragma unroll 1
    for (unsigned n = 0; n < SPIN_CAP; ++n) {
      a = ld_gran(ga); ml = ld_gran(gm);
      if (__all((unsigned)(a >> 32) == T && (unsigned)(ml >> 32) == T)) break;
    }
    const float mlv = __uint_as_float((unsigned)ml), oth = dpp<XOR1>(mlv);
    const float Mp = (j & 1) ? oth : mlv, Lp = (j & 1) ? mlv : oth;
    const float M = x32_max(x16_max(Mp));
    const float wt = __builtin_amdgcn_exp2f(Mp - M);
    const float L = x32_sum(x16_sum(wt * Lp));
    const float o = x32_sum(x16_sum(wt * __uint_as_float((unsigned)a)));
    const unsigned ob = f2bf(o / L);
    const unsigned pk = ob | (__builtin_bit_cast(unsigned, dpp<XOR1>(__builtin_bit_cast(float, ob))) << 16);
    float sa[8];
#pragma unroll
    for (int q = 0; q < 8; ++q) sa[q] = 0.f;
#pragma unroll
    for (int i = 0; i < 8; ++i) {
      const unsigned op = __builtin_amdgcn_readlane(pk, 2 * i);
#pragma unroll
      for (int q = 0; q < 4; ++q) { sa[2 * q] = dot2(wor[2 * i][q], op, sa[2 * q]); sa[2 * q + 1] = dot2(wor[2 * i + 1][q], op, sa[2 * q + 1]); }
    }
    if (lane < 56) { *(f32x4*)(wreg + 1024 + lane * 8) = f32x4{sa[0], sa[1], sa[2], sa[3]}; *(f32x4*)(wreg + 1024 + lane * 8 + 4) = f32x4{sa[4], sa[5], sa[6], sa[7]}; }
  }
  lds_barrier();
  if (tid < 448) {
    float s = 0.f, y = 0.f;
#pragma unroll
    for (int ww = 0; ww < 8; ++ww) s += lds[ww * L_WREG + 1024 + tid];
#pragma unroll
    for (int ww = 0; ww < 7; ++ww) y += lds[L_Y + ww * 448 + tid];
    __builtin_amdgcn_raw_buffer_store_b128(v4u{__float_as_uint(s), T, __float_as_uint(y), T}, rs, WS_SG + (unsigned)(b * 32 + part) * (448 * 16) + tid * 16, 0, 16);
  }
  if (part < 28) {
    const int pp = tid >> 4, col = tid & 15;
    const unsigned off = WS_SG + (unsigned)(b * 32 + pp) * (448 * 16) + (part * 16 + col) * 16;
    v4u g;
#pragma unroll 1
    for (unsigned n = 0; n < SPIN_CAP; ++n) {
      asm volatile("" ::: "memory");
      g = __builtin_amdgcn_raw_buffer_load_b128(rs, off, 0, 16);
      if (__all(g.y == T && g.w == T)) break;
    }
    const float s = x32_sum(x16_sum(__uint_as_float(g.x))), y = x32_sum(x16_sum(__uint_as_float(g.z)));
    if (lane < 16) *(f32x2*)(wreg + 1536 + lane * 2) = f32x2{s, y};
    lds_barrier();
    if (tid < 16) {
      float st = 0.f, yt = 0.f;
#pragma unroll
      for (int ww = 0; ww < 8; ++ww) { st += lds[ww * L_WREG + 1536 + tid * 2]; yt += lds[ww * L_WREG + 1536 + tid * 2 + 1]; }
      st = rbf(st);
      yt = rbf(rbf(yt) + b2v);
      const float g2 = rbf(yt / (1.f + __expf(-yt)));
      P.out[b * D + part * 16 + tid] = (unsigned short)f2bf(xin + rbf(st * g2));
    }
  }
}

extern "C" void kernel_launch(void* const* d_in, const int* in_sizes, int n_in, void* d_out, int out_size, void* d_ws, size_t ws_size,
                              hipStream_t stream) {
  Params P;
  P.x = (const unsigned short*)d_in[0];
  P.kc = (const unsigned short*)d_in[1];
  P.vc = (const unsigned short*)d_in[2];
  P.wq = (const unsigned short*)d_in[3];
  P.wo = (const unsigned short*)d_in[4];
  P.w1 = (const unsigned short*)d_in[6];
  P.b1 = (const unsigned short*)d_in[7];
  P.w2 = (const unsigned short*)d_in[8];
  P.b2 = (const unsigned short*)d_in[9];
  P.out = (unsigned short*)d_out;
  P.ws = (unsigned char*)d_ws;
  (void)hipFuncSetAttribute((const void*)fused_kernel, hipFuncAttributeMaxDynamicSharedMemorySize, LDS_BYTES);
  fused_kernel<<<NWG, NTHR, LDS_BYTES, stream>>>(P);
}
```

```cpp
#include <hip/hip_runtime.h>
#include <hip/hip_bf16.h>
#include <stdint.h>

typedef unsigned v4u __attribute__((ext_vector_type(4)));
typedef float f32x4 __attribute__((ext_vector_type(4)));
typedef float f32x2 __attribute__((ext_vector_type(2)));
typedef __attribute__((ext_vector_type(2))) __bf16 bf16x2;
typedef unsigned long long u64;
#define GAS __attribute__((address_space(1)))
#define RLX_AGENT __ATOMIC_RELAXED, __HIP_MEMORY_SCOPE_AGENT

constexpr int B = 8, D = 448, HKV = 8, SK = 8192, DH = 256, F = 1792;
constexpr int NWG = 256, NW = 8, NTHR = 512;
constexpr int GS = 32;
constexpr unsigned SPIN_CAP = 1u << 17;
constexpr unsigned OOB = 0x80000000u;

constexpr unsigned WS_EPOCH = 0;
constexpr unsigned WS_QG = 4096;
constexpr unsigned WS_HG = WS_QG + 8 * 8 * 4 * 512 * 8;
constexpr unsigned WS_PG = WS_HG + 8 * 8 * 1792 * 8;
constexpr unsigned PG_STRIDE = 576;
constexpr unsigned WS_SG = WS_PG + 256 * PG_STRIDE * 8;
constexpr unsigned WS_END = WS_SG + 8 * 32 * 448 * 16;

__device__ __forceinline__ float bf2f(unsigned short b) { return __builtin_bit_cast(float, (unsigned)b << 16); }
__device__ __forceinline__ float rbf(float f) { return (float)(__bf16)f; }
__device__ __forceinline__ unsigned f2bf(float f) { return (unsigned)__builtin_bit_cast(unsigned short, (__bf16)f); }
__device__ __forceinline__ float lo16(unsigned u) { return __builtin_bit_cast(float, u << 16); }
__device__ __forceinline__ float hi16(unsigned u) { return __builtin_bit_cast(float, u & 0xffff0000u); }
__device__ __forceinline__ float dot2(unsigned k, unsigned q, float acc) {
  return __builtin_amdgcn_fdot2_f32_bf16(__builtin_bit_cast(bf16x2, k), __builtin_bit_cast(bf16x2, q), acc, false);
}
template <int CTRL> __device__ __forceinline__ float dpp(float x) {
  return __builtin_bit_cast(float, __builtin_amdgcn_mov_dpp(__builtin_bit_cast(int, x), CTRL, 0xf, 0xf, true));
}
constexpr int XOR1 = 0xB1, XOR2 = 0x4E, XOR7 = 0x141, XOR8 = 0x128;
__device__ __forceinline__ float x16_sum(float x) {
  auto s = __builtin_amdgcn_permlane16_swap(__float_as_uint(x), __float_as_uint(x), false, false);
  return __uint_as_float(s[0]) + __uint_as_float(s[1]);
}
__device__ __forceinline__ float x32_sum(float x) {
  auto s = __builtin_amdgcn_permlane32_swap(__float_as_uint(x), __float_as_uint(x), false, false);
  return __uint_as_float(s[0]) + __uint_as_float(s[1]);
}
__device__ __forceinline__ float x16_max(float x) {
  auto s = __builtin_amdgcn_permlane16_swap(__float_as_uint(x), __float_as_uint(x), false, false);
  return fmaxf(__uint_as_float(s[0]), __uint_as_float(s[1]));
}
__device__ __forceinline__ float x32_max(float x) {
  auto s = __builtin_amdgcn_permlane32_swap(__float_as_uint(x), __float_as_uint(x), false, false);
  return fmaxf(__uint_as_float(s[0]), __uint_as_float(s[1]));
}
__device__ __forceinline__ float tr32(float a, float b) {
  auto s = __builtin_amdgcn_permlane32_swap(__float_as_uint(a), __float_as_uint(b), false, false);
  return __uint_as_float(s[0]) + __uint_as_float(s[1]);
}
__device__ __forceinline__ float wave_sum(float x) {
  x += dpp<XOR1>(x); x += dpp<XOR2>(x); x += dpp<XOR7>(x); x += dpp<XOR8>(x); x = x16_sum(x); return x32_sum(x);
}
__device__ __forceinline__ v4u ldnt(const GAS v4u* p) { return __builtin_nontemporal_load(p); }
__device__ __forceinline__ void st_gran(GAS u64* g, unsigned tag, unsigned val) { __hip_atomic_store(g, ((u64)tag << 32) | val, RLX_AGENT); }
__device__ __forceinline__ u64 ld_gran(GAS u64* g) { return __hip_atomic_load(g, RLX_AGENT); }
__device__ __forceinline__ void lds_barrier() { asm volatile("s_waitcnt lgkmcnt(0)" ::: "memory"); __builtin_amdgcn_s_barrier(); asm volatile("" ::: "memory"); }
__device__ __forceinline__ __amdgpu_buffer_rsrc_t mk_rsrc(const void* p, unsigned bytes) { return __builtin_amdgcn_make_buffer_rsrc((void*)p, 0, bytes, 0x00020000); }
__device__ __forceinline__ void glds16(const void* gsrc, unsigned lds_dst) {
  unsigned keep;
  asm volatile("s_mov_b32 %0, m0\n\ts_mov_b32 m0, %2\n\ts_nop 0\n\tglobal_load_lds_dwordx4 %1, off nt\n\ts_mov_b32 m0, %0" : "=&s"(keep) : "v"(gsrc), "s"(lds_dst) : "memory");
}

struct Params {
  const unsigned short *x, *kc, *vc, *wq, *wo, *w1, *b1, *w2, *b2;
  unsigned short* out;
  unsigned char* ws;
};

constexpr int L_WREG = 2048;
constexpr int L_XNP = 8 * L_WREG;
constexpr int L_Q = L_XNP + 2016;
constexpr int L_Y = L_Q + 256;
constexpr int L_SC = L_Y + 7 * 448;
constexpr int NSLOT = 8 * GS;
constexpr int L_CTR = L_SC + NSLOT * 16;
constexpr int L_END = L_CTR + 4;
constexpr int LDS_BYTES = L_END * 4;
static_assert(LDS_BYTES <= 160 * 1024 && LDS_BYTES > 80 * 1024 && L_Y % 4 == 0 && L_Q % 4 == 0 && L_SC % 4 == 0, "");

__global__ void __launch_bounds__(NTHR, 2) fused_kernel(Params P) {
  extern __shared__ __attribute__((aligned(16))) float lds[];
  unsigned* lds_u = (unsigned*)lds;
  const int tid = threadIdx.x, lane = tid & 63, w = __builtin_amdgcn_readfirstlane(tid >> 6);
  const int bid = blockIdx.x;
  const int pair = bid >> 2, sp = bid & 3, b = pair >> 3, kvh = pair & 7;
  const int part = kvh * 4 + sp;
  const int c = lane & 31;
  GAS unsigned char* ws = (GAS unsigned char*)P.ws;
  GAS u64* qg = (GAS u64*)(ws + WS_QG);
  GAS u64* hg = (GAS u64*)(ws + WS_HG);
  GAS u64* pg = (GAS u64*)(ws + WS_PG);
  const __amdgpu_buffer_rsrc_t rs = mk_rsrc(P.ws, WS_END);
  float* wreg = lds + w * L_WREG;
  unsigned* wreg_u = lds_u + w * L_WREG;
  const bool has_f1 = (bid & 7) != 7;
  const int f1u = (bid >> 3) * 7 + (bid & 7), f1cb = f1u >> 3, f1kq = f1u & 7;
  const unsigned epoch = __hip_atomic_load((GAS unsigned*)(ws + WS_EPOCH), RLX_AGENT);
  const unsigned l56 = lane < 56 ? lane * 16 : OOB;
  const v4u xv = __builtin_amdgcn_raw_buffer_load_b128(mk_rsrc(P.x, B * D * 2), l56 + w * (D * 2), 0, 2);
  unsigned wl[4], wh[4];
  {
    const __amdgpu_buffer_rsrc_t r = mk_rsrc(P.wq, D * 4096 * 2);
    const unsigned o0 = tid < 448 ? (unsigned)((sp * 112 + 2 * (w * 2 + (lane >> 5))) * 4096 + kvh * 512 + b * 64) * 2 + c * 4 : OOB;
#pragma unroll
    for (int j = 0; j < 4; ++j) {
      wl[j] = __builtin_amdgcn_raw_buffer_load_b32(r, o0 + (unsigned)(28 * j) * 8192, 0, 2);
      wh[j] = __builtin_amdgcn_raw_buffer_load_b32(r, o0 + (unsigned)(28 * j + 1) * 8192, 0, 2);
    }
  }
  {
    const v4u z = {0u, 0u, 0u, 0u};
    if (tid < (int)PG_STRIDE / 2) __builtin_amdgcn_raw_buffer_store_b128(z, rs, WS_PG + (unsigned)bid * (PG_STRIDE * 8) + tid * 16, 0, 16);
    if (tid < 448) __builtin_amdgcn_raw_buffer_store_b128(z, rs, WS_SG + (unsigned)(b * 32 + part) * (448 * 16) + tid * 16, 0, 16);
    if (has_f1 && tid < 256) __builtin_amdgcn_raw_buffer_store_b128(z, rs, WS_HG + (unsigned)((((tid >> 5) * 8 + f1kq) * 1792 + f1cb * 64) * 8) + (tid & 31) * 16, 0, 16);
    if (tid == 0) lds_u[L_CTR] = 0u;
  }
  __builtin_amdgcn_sched_barrier(0);
  __builtin_amdgcn_s_barrier();
  const size_t base = (size_t)pair * SK * DH;
  constexpr int USTR = NW * 64, TSTR = NW * 256;
  const GAS v4u* kbase = (const GAS v4u*)(P.kc + base) + lane;
  const GAS v4u* vbase = (const GAS v4u*)(P.vc + base) + lane;
  const GAS v4u* kp = kbase + (size_t)sp * GS * TSTR + w * 64;
  v4u K0[4], K1[4], K2[4], K3[4], K4[4], K5[4], K6[4], K7[4], Kb[4];
#define LD4(dst, ptr) { _Pragma("unroll") for (int u = 0; u < 4; ++u) dst[u] = ldnt(ptr + u * USTR); ptr += TSTR; __builtin_amdgcn_sched_barrier(0); }
  LD4(K0, kp)
  {
    float v[8];
#pragma unroll
    for (int j = 0; j < 4; ++j) { v[2 * j] = lo16(xv[j]); v[2 * j + 1] = hi16(xv[j]); }
    asm volatile("" : "+v"(v[0]), "+v"(v[1]), "+v"(v[2]), "+v"(v[3]), "+v"(v[4]), "+v"(v[5]), "+v"(v[6]), "+v"(v[7]));
    float s = 0.f;
#pragma unroll
    for (int j = 0; j < 8; ++j) s += v[j];
    const float mean = wave_sum(s) * (1.f / D);
    float q = 0.f;
#pragma unroll
    for (int j = 0; j < 8; ++j) { const float d = v[j] - mean; q = fmaf(d, d, q); }
    if (lane >= 56) q = 0.f;
    const float var = rbf(wave_sum(q) * (1.f / D));
    const float mb = rbf(mean);
    const float inv = 1.f / rbf(sqrtf(rbf(var + 1e-5f)));
    if (lane < 56) {
#pragma unroll
      for (int j = 0; j < 4; ++j) {
        const unsigned lo = f2bf(rbf(v[2 * j] - mb) * inv), hi = f2bf(rbf(v[2 * j + 1] - mb) * inv);
        lds_u[L_XNP + (lane * 4 + j) * 8 + lane * 4 + w] = lo | (hi << 16);
      }
    }
  }
  lds_barrier();
  if (w < 7) {
    float x[16];
#pragma unroll
    for (int v = 0; v < 16; ++v) x[v] = 0.f;
#pragma unroll
    for (int j = 0; j < 4; ++j) {
      const int kq = sp * 56 + w * 2 + (lane >> 5) + 14 * j;
      const unsigned* xr = lds_u + L_XNP + kq * 8 + (kq >> 2) * 4;
      const v4u xa = *(const v4u*)xr, xb = *(const v4u*)(xr + 4);
      const unsigned pa = __builtin_amdgcn_perm(wh[j], wl[j], 0x05040100u), pb = __builtin_amdgcn_perm(wh[j], wl[j], 0x07060302u);
#pragma unroll
      for (int bb = 0; bb < 4; ++bb) { x[bb] = dot2(pa, xa[bb], x[bb]); x[4 + bb] = dot2(pa, xb[bb], x[4 + bb]); x[8 + bb] = dot2(pb, xa[bb], x[8 + bb]); x[12 + bb] = dot2(pb, xb[bb], x[12 + bb]); }
    }
#pragma unroll
    for (int u = 0; u < 8; ++u) wreg[u * 64 + 2 * c + (lane >> 5)] = tr32(x[u], x[u + 8]);
  }
  asm volatile("s_waitcnt vmcnt(4)" ::: "memory");
  LD4(K1, kp)
  lds_barrier();
  const unsigned T = (epoch + 1u) | 0x80000000u;
  {
    float v0 = 0.f;
#pragma unroll
    for (int wv = 0; wv < 7; ++wv) v0 += lds[wv * L_WREG + w * 64 + lane];
    st_gran(qg + ((kvh * 8 + w) * 4 + sp) * 512 + b * 64 + lane, T, __float_as_uint(v0));
  }
  if (w == 0) {
    const unsigned qoff = WS_QG + (unsigned)(kvh * 8 + b) * 16384 + lane * 16;
    v4u g[16];
    for (unsigned n = 0; n < SPIN_CAP; ++n) {
      asm volatile("" ::: "memory");
#pragma unroll
      for (int i = 0; i < 16; ++i) g[i] = __builtin_amdgcn_raw_buffer_load_b128(rs, qoff + i * 1024, 0, 16);
      bool ok = true;
#pragma unroll
      for (int i = 0; i < 16; ++i) ok &= (g[i].y == T) & (g[i].w == T);
      if (__all(ok)) break;
    }
#pragma unroll
    for (int cc = 0; cc < 4; ++cc) {
      const float q0 = ((__uint_as_float(g[cc].x) + __uint_as_float(g[4 + cc].x)) + __uint_as_float(g[8 + cc].x)) + __uint_as_float(g[12 + cc].x);
      const float q1 = ((__uint_as_float(g[cc].z) + __uint_as_float(g[4 + cc].z)) + __uint_as_float(g[8 + cc].z)) + __uint_as_float(g[12 + cc].z);
      lds_u[L_Q + cc * 64 + lane] = f2bf(q0) | (f2bf(q1) << 16);
    }
    if (lane == 0) __hip_atomic_store(lds_u + L_CTR, 1u, __ATOMIC_RELAXED, __HIP_MEMORY_SCOPE_WORKGROUP);
  }
#define PACE() { if (__builtin_amdgcn_readfirstlane(__hip_atomic_load(lds_u + L_CTR, __ATOMIC_RELAXED, __HIP_MEMORY_SCOPE_WORKGROUP)) == 0u) asm volatile("s_waitcnt vmcnt(0)" ::: "memory"); }
  PACE() LD4(K2, kp)
  PACE() LD4(K3, kp)
  PACE() LD4(K4, kp)
  PACE() LD4(K5, kp)
  PACE() LD4(K6, kp)
  PACE() LD4(K7, kp)
  lds_barrier();
  v4u qv[2];
  qv[0] = *(const v4u*)(lds_u + L_Q + c * 4);
  qv[1] = *(const v4u*)(lds_u + L_Q + 128 + c * 4);

  constexpr float C2 = 0.0625f * 1.4426950408889634f;
  float mx = -INFINITY;
  int slot = w;
  const int sidx = (lane >> 5) * 8 + ((lane >> 1) & 7);
  const bool swr = (lane & 0x11) == 0;
#define TR_STEP(HALF, CTRL, BIT) _Pragma("unroll") for (int u = 0; u < (HALF); ++u) { \
        const float keep = (lane & (BIT)) ? x[u + (HALF)] : x[u]; const float send = (lane & (BIT)) ? x[u] : x[u + (HALF)]; \
        x[u] = keep + dpp<CTRL>(send); }
#define QK(KX) { float x[8]; \
      _Pragma("unroll") for (int h = 0; h < 2; ++h) \
        _Pragma("unroll") for (int i = 0; i < 4; ++i) { float d = 0.f; \
          _Pragma("unroll") for (int k = 0; k < 4; ++k) d = dot2(KX[i][k], qv[h][k], d); \
          x[h * 4 + i] = d; } \
      TR_STEP(4, XOR8, 8) TR_STEP(2, XOR7, 4) TR_STEP(1, XOR2, 2) \
      float s = x[0]; s += dpp<XOR1>(s); s = x16_sum(s); \
      s = rbf(s) * C2; \
      mx = fmaxf(mx, s); \
      if (swr) lds[L_SC + slot * 16 + sidx] = s; \
      slot += 8; \
      __builtin_amdgcn_sched_barrier(0); }
#define SEGA(N) _Pragma("unroll 1") for (int t = 0; t < (N); ++t) { LD4(Kb, kp) QK(Kb) }
#define DMA2() { glds16(dsrc, dlds); glds16(dsrc + dstr, dlds + 1024); dsrc += 2 * dstr; dlds += 2048; }
#define SEGAD(N) _Pragma("unroll 1") for (int t = 0; t < (N); ++t) { DMA2() LD4(Kb, kp) QK(Kb) }
  QK(K0) QK(K1) QK(K2) QK(K3)
  LD4(Kb, kp)
  QK(K4) QK(K5) QK(K6) QK(K7)
  QK(Kb)
  const int l55 = lane < 56 ? lane : 55;
  {
    const char* dsrc; size_t dstr; unsigned dlds = (unsigned)w * (L_WREG * 4);
    if (w < 7) { dsrc = (const char*)(P.w2 + (size_t)(56 * part + 8 * w) * D + l55 * 8); dstr = D * 2; }
    else { dsrc = (const char*)(P.w1 + (size_t)(56 * f1kq + (lane >> 3)) * F + (has_f1 ? f1cb : 0) * 64 + (lane & 7) * 8); dstr = has_f1 ? (size_t)8 * F * 2 : 0; }
    SEGAD(3)
    if (w == 7) dstr = 0;
    SEGAD(1)
  }
  LD4(Kb, kp)
  if (w == 7 && has_f1) {
    float x[16];
#pragma unroll
    for (int v = 0; v < 16; ++v) x[v] = 0.f;
#pragma unroll 2
    for (int j = 0; j < 14; ++j) {
      const int kl = (lane >> 5) + 2 * j, kq = f1kq * 28 + kl;
      const unsigned* xr = lds_u + L_XNP + kq * 8 + (kq >> 2) * 4;
      const v4u xa = *(const v4u*)xr, xb = *(const v4u*)(xr + 4);
      const unsigned a0 = wreg_u[(2 * kl) * 32 + c], a1 = wreg_u[(2 * kl + 1) * 32 + c];
      const unsigned pa = __builtin_amdgcn_perm(a1, a0, 0x05040100u), pb = __builtin_amdgcn_perm(a1, a0, 0x07060302u);
#pragma unroll
      for (int bb = 0; bb < 4; ++bb) { x[bb] = dot2(pa, xa[bb], x[bb]); x[4 + bb] = dot2(pa, xb[bb], x[4 + bb]); x[8 + bb] = dot2(pb, xa[bb], x[8 + bb]); x[12 + bb] = dot2(pb, xb[bb], x[12 + bb]); }
    }
    GAS u64* hd = hg + (size_t)f1kq * 1792 + f1cb * 64 + 2 * c + (lane >> 5);
#pragma unroll
    for (int u = 0; u < 8; ++u) st_gran(hd + (size_t)u * (8 * 1792), T, __float_as_uint(tr32(x[u], x[u + 8])));
  }
  QK(Kb)
  SEGA(5)
  {
    GAS u64* rgp[4]; u64 rg[4];
    const int hcol = 56 * part + 8 * (w < 7 ? w : 0) + (lane & 7);
    {
      const int pu = (hcol >> 6) * 8 + (lane >> 3), pbid = (pu / 7) * 8 + pu % 7;
#pragma unroll
      for (int j = 0; j < 4; ++j) {
        const int i = w < 7 ? pbid : lane * 4 + j;
        rgp[j] = (w < 7 && j == 0) ? hg + (size_t)(b * 8 + (lane >> 3)) * 1792 + hcol : qg + (size_t)((((i >> 2) & 7) * 8) * 4 + (i & 3)) * 512 + (i >> 5) * 64;
        rg[j] = ld_gran(rgp[j]);
      }
    }
    const float b1v = bf2f(P.b1[hcol]);
    SEGA(1)
    LD4(Kb, kp)
    asm volatile("s_waitcnt vmcnt(4)" : "+v"(rg[0]), "+v"(rg[1]), "+v"(rg[2]), "+v"(rg[3]));
    for (unsigned n = 0; n < SPIN_CAP; ++n) {
      if (__all((unsigned)(rg[0] >> 32) == T && (unsigned)(rg[1] >> 32) == T && (unsigned)(rg[2] >> 32) == T && (unsigned)(rg[3] >> 32) == T)) break;
      __builtin_amdgcn_s_sleep(1);
#pragma unroll
      for (int j = 0; j < 4; ++j) rg[j] = ld_gran(rgp[j]);
    }
    if (w < 7) {
      float hv = __uint_as_float((unsigned)rg[0]);
      hv += dpp<XOR8>(hv); hv = x16_sum(hv); hv = x32_sum(hv);
      const float t = rbf(rbf(hv) + b1v);
      const float u = 0.7978845608028654f * (t + 0.044715f * t * t * t);
      const float th = 1.f - 2.f / (__expf(2.f * u) + 1.f);
      const unsigned hb = f2bf(0.5f * t * (1.f + th));
      const unsigned hpk = hb | (__builtin_bit_cast(unsigned, dpp<XOR1>(__builtin_bit_cast(float, hb))) << 16);
      float ya[8];
#pragma unroll
      for (int j = 0; j < 8; ++j) ya[j] = 0.f;
#pragma unroll
      for (int i = 0; i < 4; ++i) {
        const unsigned hp = __builtin_amdgcn_readlane(hpk, 2 * i);
        const v4u r0 = *(const v4u*)(wreg_u + (2 * i) * 256 + lane * 4), r1 = *(const v4u*)(wreg_u + (2 * i + 1) * 256 + lane * 4);
#pragma unroll
        for (int q = 0; q < 4; ++q) {
          ya[2 * q] = dot2(__builtin_amdgcn_perm(r1[q], r0[q], 0x05040100u), hp, ya[2 * q]);
          ya[2 * q + 1] = dot2(__builtin_amdgcn_perm(r1[q], r0[q], 0x07060302u), hp, ya[2 * q + 1]);
        }
      }
      if (lane < 56) { *(f32x4*)(lds + L_Y + w * 448 + lane * 8) = f32x4{ya[0], ya[1], ya[2], ya[3]}; *(f32x4*)(lds + L_Y + w * 448 + lane * 8 + 4) = f32x4{ya[4], ya[5], ya[6], ya[7]}; }
    } else {
      if (bid == 0 && lane == 0) __hip_atomic_store((GAS unsigned*)(ws + WS_EPOCH), epoch + 1u, RLX_AGENT);
    }
    asm volatile("s_waitcnt lgkmcnt(0)" ::: "memory");
  }
  QK(Kb)
  SEGA(GS - 21)
  v4u VA[4], VB[4], Vb[4];
  const GAS v4u* vp = vbase + (size_t)sp * GS * TSTR + w * 64;
  LD4(VA, vp) LD4(VB, vp)
  mx = fmaxf(mx, dpp<XOR2>(mx)); mx = fmaxf(mx, dpp<XOR7>(mx)); mx = x32_max(mx);
  const float M = mx;
  float acc[2][8];
#pragma unroll
  for (int h = 0; h < 2; ++h)
#pragma unroll
    for (int d = 0; d < 8; ++d) acc[h][d] = 0.f;
  float l = 0.f;
#define PV1(VX, I) { float vf[8]; _Pragma("unroll") for (int k = 0; k < 4; ++k) { vf[2 * k] = lo16(VX[I][k]); vf[2 * k + 1] = hi16(VX[I][k]); } \
        const float p0 = dpp<0x150 + 2 * I>(p), p1 = dpp<0x158 + 2 * I>(p); \
        _Pragma("unroll") for (int d = 0; d < 8; ++d) { acc[0][d] = fmaf(p0, vf[d], acc[0][d]); acc[1][d] = fmaf(p1, vf[d], acc[1][d]); } }
#define PV(VX, SL) { const float p = __builtin_amdgcn_exp2f(lds[L_SC + (SL) * 16 + sidx] - M); l += p; \
        PV1(VX, 0) PV1(VX, 1) PV1(VX, 2) PV1(VX, 3) __builtin_amdgcn_sched_barrier(0); }
#define BTILE() { LD4(Vb, vp) PV(Vb, cur) cur += 8; }
  int cur = 16 + w;
  PV(VA, w) PV(VB, 8 + w)
  v4u wor[16];
  {
    const char* dsrc = (const char*)(P.wo + (size_t)((kvh * 2 + (sp >> 1)) * 256 + (sp & 1) * 128 + 16 * w) * D + l55 * 8);
    const size_t dstr = D * 2;
#pragma unroll
    for (int hf = 0; hf < 2; ++hf) {
      unsigned dlds = (unsigned)w * (L_WREG * 4);
#pragma unroll 1
      for (int t = 0; t < 4; ++t) { DMA2() BTILE() }
      asm volatile("s_waitcnt vmcnt(0)" ::: "memory");
#pragma unroll
      for (int i = 0; i < 4; ++i) {
        const v4u r0 = *(const v4u*)(wreg_u + (2 * i) * 256 + lane * 4), r1 = *(const v4u*)(wreg_u + (2 * i + 1) * 256 + lane * 4);
#pragma unroll
        for (int q = 0; q < 4; ++q) {
          wor[8 * hf + 2 * i][q] = __builtin_amdgcn_perm(r1[q], r0[q], 0x05040100u);
          wor[8 * hf + 2 * i + 1][q] = __builtin_amdgcn_perm(r1[q], r0[q], 0x07060302u);
        }
      }
#pragma unroll
      for (int r = 0; r < 8; ++r) asm volatile("" : "+v"(wor[8 * hf + r]));
      asm volatile("s_waitcnt lgkmcnt(0)" ::: "memory");
    }
  }
  float xin = 0.f, b2v = 0.f;
  if (part < 28 && tid < 16) { xin = bf2f(P.x[b * D + part * 16 + tid]); b2v = bf2f(P.b2[part * 16 + tid]); }
#pragma unroll 1
  for (int t = 0; t < GS - 10; ++t) BTILE()
#pragma unroll
  for (int h = 0; h < 2; ++h)
#pragma unroll
    for (int d = 0; d < 8; ++d) acc[h][d] = x32_sum(acc[h][d]);
  l += dpp<XOR2>(l); l += dpp<XOR7>(l); l = x32_sum(l);
  if (lane < 32) {
#pragma unroll
    for (int h = 0; h < 2; ++h) {
      *(f32x4*)(wreg + h * 260 + 4 + c * 8) = f32x4{acc[h][0], acc[h][1], acc[h][2], acc[h][3]};
      *(f32x4*)(wreg + h * 260 + 4 + c * 8 + 4) = f32x4{acc[h][4], acc[h][5], acc[h][6], acc[h][7]};
    }
    if ((lane & 7) == 0 && lane < 16) { wreg[(lane >> 3) * 260] = M; wreg[(lane >> 3) * 260 + 1] = l; }
  }
  lds_barrier();
  {
    const int h = tid >> 8, d = tid & 255;
    const float* mrg = lds + h * 260;
    float Mx = -INFINITY;
#pragma unroll
    for (int ww = 0; ww < NW; ++ww) Mx = fmaxf(Mx, mrg[ww * L_WREG]);
    float L = 0.f, o = 0.f;
#pragma unroll
    for (int ww = 0; ww < NW; ++ww) { const float wt = __builtin_amdgcn_exp2f(mrg[ww * L_WREG] - Mx); L = fmaf(wt, mrg[ww * L_WREG + 1], L); o = fmaf(wt, mrg[ww * L_WREG + 4 + d], o); }
    GAS u64* slotp = pg + (size_t)bid * PG_STRIDE;
    st_gran(slotp + h * 256 + d, T, __float_as_uint(o));
    if (d < 2) st_gran(slotp + 512 + 2 * h + d, T, __float_as_uint(d == 0 ? Mx : L));
  }
  {
    const int hs = sp >> 1, pj = lane >> 4, j = lane & 15;
    GAS u64* slot = pg + (size_t)(pair * 4 + pj) * PG_STRIDE;
    GAS u64* ga = slot + hs * 256 + (sp & 1) * 128 + 16 * w + j;
    GAS u64* gm = slot + 512 + 2 * hs + (j & 1);
    u64 a, ml;
#pragma unroll 1
    for (unsigned n = 0; n < SPIN_CAP; ++n) {
      a = ld_gran(ga); ml = ld_gran(gm);
      if (__all((unsigned)(a >> 32) == T && (unsigned)(ml >> 32) == T)) break;
    }
    const float mlv = __uint_as_float((unsigned)ml), oth = dpp<XOR1>(mlv);
    const float Mp = (j & 1) ? oth : mlv, Lp = (j & 1) ? mlv : oth;
    const float M = x32_max(x16_max(Mp));
    const float wt = __builtin_amdgcn_exp2f(Mp - M);
    const float L = x32_sum(x16_sum(wt * Lp));
    const float o = x32_sum(x16_sum(wt * __uint_as_float((unsigned)a)));
    const unsigned ob = f2bf(o / L);
    const unsigned pk = ob | (__builtin_bit_cast(unsigned, dpp<XOR1>(__builtin_bit_cast(float, ob))) << 16);
    float sa[8];
#pragma unroll
    for (int q = 0; q < 8; ++q) sa[q] = 0.f;
#pragma unroll
    for (int i = 0; i < 8; ++i) {
      const unsigned op = __builtin_amdgcn_readlane(pk, 2 * i);
#pragma unroll
      for (int q = 0; q < 4; ++q) { sa[2 * q] = dot2(wor[2 * i][q], op, sa[2 * q]); sa[2 * q + 1] = dot2(wor[2 * i + 1][q], op, sa[2 * q + 1]); }
    }
    if (lane < 56) { *(f32x4*)(wreg + 1024 + lane * 8) = f32x4{sa[0], sa[1], sa[2], sa[3]}; *(f32x4*)(wreg + 1024 + lane * 8 + 4) = f32x4{sa[4], sa[5], sa[6], sa[7]}; }
  }
  lds_barrier();
  if (tid < 448) {
    float s = 0.f, y = 0.f;
#pragma unroll
    for (int ww = 0; ww < 8; ++ww) s += lds[ww * L_WREG + 1024 + tid];
#pragma unroll
    for (int ww = 0; ww < 7; ++ww) y += lds[L_Y + ww * 448 + tid];
    __builtin_amdgcn_raw_buffer_store_b128(v4u{__float_as_uint(s), T, __float_as_uint(y), T}, rs, WS_SG + (unsigned)(b * 32 + part) * (448 * 16) + tid * 16, 0, 16);
  }
  if (part < 28) {
    const int pp = tid >> 4, col = tid & 15;
    const unsigned off = WS_SG + (unsigned)(b * 32 + pp) * (448 * 16) + (part * 16 + col) * 16;
    v4u g;
#pragma unroll 1
    for (unsigned n = 0; n < SPIN_CAP; ++n) {
      asm volatile("" ::: "memory");
      g = __builtin_amdgcn_raw_buffer_load_b128(rs, off, 0, 16);
      if (__all(g.y == T && g.w == T)) break;
    }
    const float s = x32_sum(x16_sum(__uint_as_float(g.x))), y = x32_sum(x16_sum(__uint_as_float(g.z)));
    if (lane < 16) *(f32x2*)(wreg + 1536 + lane * 2) = f32x2{s, y};
    lds_barrier();
    if (tid < 16) {
      float st = 0.f, yt = 0.f;
#pragma unroll
      for (int ww = 0; ww < 8; ++ww) { st += lds[ww * L_WREG + 1536 + tid * 2]; yt += lds[ww * L_WREG + 1536 + tid * 2 + 1]; }
      st = rbf(st);
      yt = rbf(rbf(yt) + b2v);
      const float g2 = rbf(yt / (1.f + __expf(-yt)));
      P.out[b * D + part * 16 + tid] = (unsigned short)f2bf(xin + rbf(st * g2));
    }
  }
}

extern "C" void kernel_launch(void* const* d_in, const int* in_sizes, int n_in, void* d_out, int out_size, void* d_ws, size_t ws_size,
                              hipStream_t stream) {
  Params P;
  P.x = (const unsigned short*)d_in[0];
  P.kc = (const unsigned short*)d_in[1];
  P.vc = (const unsigned short*)d_in[2];
  P.wq = (const unsigned short*)d_in[3];
  P.wo = (const unsigned short*)d_in[4];
  P.w1 = (const unsigned short*)d_in[6];
  P.b1 = (const unsigned short*)d_in[7];
  P.w2 = (const unsigned short*)d_in[8];
  P.b2 = (const unsigned short*)d_in[9];
  P.out = (unsigned short*)d_out;
  P.ws = (unsigned char*)d_ws;
  (void)hipFuncSetAttribute((const void*)fused_kernel, hipFuncAttributeMaxDynamicSharedMemorySize, LDS_BYTES);
  fused_kernel<<<NWG, NTHR, LDS_BYTES, stream>>>(P);
}
```
